```python
import torch

def _softmax(x, axis=-1):
    return torch.softmax(x, dim=axis)
def _gelu(x):
    return torch.nn.functional.gelu(x, approximate="tanh")
def _silu(x):
    return torch.nn.functional.silu(x)
def _sigmoid(x):
    return torch.sigmoid(x)
def _layernorm(x, axis=-1, eps=1e-5):
    m = x.mean(dim=axis, keepdim=True)
    v = x.var(dim=axis, keepdim=True, unbiased=False)
    return (x - m) / torch.sqrt(v + eps)
def _scatter_add(tgt, idx, src, axis=0):
    return tgt.clone().index_add_(axis, idx, src)
def _segment_sum(src, seg, n):
    out = torch.zeros((n,) + src.shape[1:], dtype=src.dtype)
    return out.index_add_(0, seg, src)
def _pad(x, pw):
    flat = tuple(v for pair in reversed(pw) for v in pair)
    return torch.nn.functional.pad(x, flat)

kernel_name = 'recurrent_scan__sliding_block_cached__glu_gated_bilinear__decode'

def setup_inputs():

    _w = (lambda a: (lambda t: t.to(torch.bfloat16) if t.is_floating_point() else t)(torch.as_tensor(a)))
    def _A_setup():
        B, Sq, D = 1, 1, 640
        Hq, Hkv, Sk, Dh = 32, 32, 8192, 192
        assert Hq % Hkv == 0, (Hq, Hkv)
        return {
            "x": _w((lambda *s: torch.randn(*s, dtype=torch.bfloat16) * 0.2)(B, Sq, D)),
            "k_cache": _w((lambda *s: torch.randn(*s, dtype=torch.bfloat16) * 1.6)(B, Hkv, Sk, Dh)),
            "v_cache": _w((lambda *s: torch.randn(*s, dtype=torch.bfloat16) * 1.6)(B, Hkv, Sk, Dh)),
            "wq": _w((lambda *s: torch.randn(*s, dtype=torch.bfloat16) * 1.6)(D, Hq * Dh) * (D ** -0.5)),
            "wo": _w((lambda *s: torch.randn(*s, dtype=torch.bfloat16) * 1.6)(Hq * Dh, D) * ((Hq * Dh) ** -0.5)),
        }
    _A_in = _A_setup()

    def _B_setup():
        B, S, D, F = 1, 1, 640, 1536
        return {
            "x": _w((lambda *s: torch.randn(*s, dtype=torch.bfloat16) * 0.2)(B, S, D)),
            "w_gate": _w((lambda *s: torch.randn(*s, dtype=torch.bfloat16) * 0.8)(D, F) * (D ** -0.5)),
            "w_up": _w((lambda *s: torch.randn(*s, dtype=torch.bfloat16) * 0.8)(D, F) * (D ** -0.5)),
            "w_down": _w((lambda *s: torch.randn(*s, dtype=torch.bfloat16) * 0.8)(F, D) * (F ** -0.5)),
        }
    _B_in = _B_setup()

    return {
        "A_x": _A_in["x"],
        "A_k_cache": _A_in["k_cache"],
        "A_v_cache": _A_in["v_cache"],
        "A_wq": _A_in["wq"],
        "A_wo": _A_in["wo"],
        "B_x": _B_in["x"],
        "B_w_gate": _B_in["w_gate"],
        "B_w_up": _B_in["w_up"],
        "B_w_down": _B_in["w_down"],
    }

def reference(A_x, A_k_cache, A_v_cache, A_wq, A_wo, B_x, B_w_gate, B_w_up, B_w_down):
    def _A_ref(x, k_cache, v_cache, wq, wo):
        B, Sq, D = x.shape
        Hq, Dh = 32, 192
        Hkv, Sk = k_cache.shape[1], k_cache.shape[2]
        W = min(4096, max(256, Sk // 4))
        rep = Hq // Hkv
        q = (lambda x, *d: x.permute(*d))((x @ wq).reshape(B, Sq, Hq, Dh), 0, 2, 1, 3)
        k = (lambda x, n, axis: torch.repeat_interleave(x, n, dim=axis))(k_cache[:, :, Sk - W:, :], rep, axis=1)
        v = (lambda x, n, axis: torch.repeat_interleave(x, n, dim=axis))(v_cache[:, :, Sk - W:, :], rep, axis=1)
        scores = torch.einsum("bhqd,bhkd->bhqk", q, k) * (Dh ** -0.5)
        p = _softmax(scores, axis=-1)
        o = torch.einsum("bhqk,bhkd->bhqd", p, v)
        return (lambda x, *d: x.permute(*d))(o, 0, 2, 1, 3).reshape(B, Sq, Hq * Dh) @ wo

    def _B_ref(x, w_gate, w_up, w_down):
        g = torch.einsum("bsd,df->bsf", x, w_gate)
        u = torch.einsum("bsd,df->bsf", x, w_up)
        h = g * u
        return torch.einsum("bsf,fd->bsd", h, w_down)

    x = A_x
    xn = _layernorm(x, axis=-1)
    s = _A_ref(xn, A_k_cache, A_v_cache, A_wq, A_wo)
    g = _silu(_B_ref(xn, B_w_gate, B_w_up, B_w_down))
    return x + s * g
```

```cpp
#include <hip/hip_runtime.h>
#include <hip/hip_bf16.h>

namespace {
typedef unsigned v4u __attribute__((ext_vector_type(4)));
typedef unsigned v3u __attribute__((ext_vector_type(3)));
typedef unsigned v2u __attribute__((ext_vector_type(2)));
typedef float f32x4 __attribute__((ext_vector_type(4)));
typedef __bf16 bf16x8 __attribute__((ext_vector_type(8)));
typedef __bf16 bf16x2 __attribute__((ext_vector_type(2)));
typedef unsigned long long u64;
typedef __amdgpu_buffer_rsrc_t rsrc_t;
#define LAS __attribute__((address_space(3)))
#define CAS __attribute__((address_space(4)))

constexpr int D = 640, H = 32, DH = 192, SK = 8192, WIN = 2048, F = 1536;
constexpr int NT = 2, SC1 = 16;
constexpr int OOB = 0x40000000;
constexpr unsigned SENT = 0xFFFFFFFFu;
constexpr unsigned SPIN_CAP = 1u << 16;

constexpr int WS_EPOCH = 0;
constexpr int WS_HELLO1 = 4096;
constexpr int WS_HELLO2 = 8192;
constexpr int WS_Q = 16384;
constexpr int WS_O = WS_Q + 256 * 1152;
constexpr int WS_S = WS_O + 256 * 1024;
constexpr int WS_END = WS_S + 160 * 1024;
constexpr int WS_STAMP = 1 << 20;

constexpr int L_RED = 0;
constexpr int L_Q = 10240;
constexpr int L_M = L_Q + 576;
constexpr int L_RED2 = L_M + 64;
constexpr int LDS_BYTES = 88 * 1024;

struct Params {
  const unsigned short* x; const unsigned char* k; const unsigned char* v; const unsigned char* wq; const unsigned char* wo;
  const unsigned char* wg; const unsigned char* wu; const unsigned char* wd; unsigned short* out; unsigned char* ws;
};

__device__ __forceinline__ rsrc_t mk(const void* p, int bytes) { return __builtin_amdgcn_make_buffer_rsrc((void*)p, 0, bytes, 0x00020000); }
__device__ __forceinline__ unsigned pk(float lo, float hi) { bf16x2 v = {(__bf16)lo, (__bf16)hi}; return __builtin_bit_cast(unsigned, v); }
__device__ __forceinline__ float rbf(float a) { return __builtin_bit_cast(float, pk(0.f, a)); }
__device__ __forceinline__ float lo16(unsigned a) { return __builtin_bit_cast(float, a << 16); }
__device__ __forceinline__ float hi16(unsigned a) { return __builtin_bit_cast(float, a & 0xffff0000u); }
__device__ __forceinline__ float asf(unsigned a) { return __builtin_bit_cast(float, a); }
__device__ __forceinline__ unsigned asu(float a) { return __builtin_bit_cast(unsigned, a); }
__device__ __forceinline__ unsigned pay(float a) { const unsigned u = asu(a); return u < SENT - 1u ? u : SENT - 1u; }
template <int CTRL> __device__ __forceinline__ float dpp(float x) {
  return __builtin_bit_cast(float, __builtin_amdgcn_mov_dpp(__builtin_bit_cast(int, x), CTRL, 0xf, 0xf, true));
}
constexpr int XOR1 = 0xB1, XOR2 = 0x4E, XOR7 = 0x141, ROR8 = 0x128;
__device__ __forceinline__ float sum8(float x) { x += dpp<XOR1>(x); x += dpp<XOR2>(x); x += dpp<XOR7>(x); return x; }
__device__ __forceinline__ float max8(float x) { x = fmaxf(x, dpp<XOR1>(x)); x = fmaxf(x, dpp<XOR2>(x)); x = fmaxf(x, dpp<XOR7>(x)); return x; }
__device__ __forceinline__ float sum_x16(float x) { auto s = __builtin_amdgcn_permlane16_swap(asu(x), asu(x), false, false); return asf(s[0]) + asf(s[1]); }
__device__ __forceinline__ float sum_x32(float x) { auto s = __builtin_amdgcn_permlane32_swap(asu(x), asu(x), false, false); return asf(s[0]) + asf(s[1]); }
__device__ __forceinline__ float max_x16(float x) { auto s = __builtin_amdgcn_permlane16_swap(asu(x), asu(x), false, false); return fmaxf(asf(s[0]), asf(s[1])); }
__device__ __forceinline__ float max_x32(float x) { auto s = __builtin_amdgcn_permlane32_swap(asu(x), asu(x), false, false); return fmaxf(asf(s[0]), asf(s[1])); }
__device__ __forceinline__ float sum_groups(float x) { x += dpp<ROR8>(x); return sum_x32(sum_x16(x)); }
__device__ __forceinline__ float dot2(unsigned a, unsigned b, float c) {
  return __builtin_amdgcn_fdot2_f32_bf16(__builtin_bit_cast(bf16x2, a), __builtin_bit_cast(bf16x2, b), c, false);
}
__device__ __forceinline__ f32x4 mfma(v4u a, v4u b, f32x4 c) {
  return __builtin_amdgcn_mfma_f32_16x16x32_bf16(__builtin_bit_cast(bf16x8, a), __builtin_bit_cast(bf16x8, b), c, 0, 0, 0);
}
__device__ __forceinline__ v4u diag(float val, v4u mask) { const unsigned pp = pk(val, val); return v4u{pp & mask.x, pp & mask.y, pp & mask.z, pp & mask.w}; }
__device__ __forceinline__ unsigned umax3(unsigned a, unsigned b, unsigned c) { const unsigned m = a > b ? a : b; return m > c ? m : c; }
__device__ __forceinline__ unsigned umax4(v4u a, unsigned m) { return umax3(a.z, a.w, umax3(a.x, a.y, m)); }
#define RELOAD() asm volatile("" ::: "memory")
#define LD4(rs, vo, so, aux) __builtin_amdgcn_raw_buffer_load_b128(rs, vo, so, aux)
#define PIN() __builtin_amdgcn_sched_barrier(0)
#define BARRIER() do { PIN(); asm volatile("s_waitcnt lgkmcnt(0)\n\ts_barrier" ::: "memory"); PIN(); } while (0)
#define STAMP(i) do { } while (0)

__global__ void __launch_bounds__(256, 1) fused_decode(Params p) {
  __shared__ __attribute__((aligned(16))) unsigned char smem[LDS_BYTES];
  LAS unsigned char* L = (LAS unsigned char*)smem;
  STAMP(0);
  const int b = blockIdx.x, tid = threadIdx.x, lane = tid & 63, w = __builtin_amdgcn_readfirstlane(tid >> 6);
  const int h = 4 * (b & 7) + ((b >> 3) & 3), s = b >> 5;
  const int g8 = lane >> 3, c8 = lane & 7, g16 = lane >> 4, c16 = lane & 15;
  const rsrc_t rws = mk(p.ws, WS_END);
  const u64 tag = *(const CAS u64*)(p.ws + WS_EPOCH) + 0x9E3779B97F4A7C15ull;

  const rsrc_t rx = mk(p.x, D * 2);
  const v4u xa = LD4(rx, lane * 20, 0, 0);
  const unsigned xb = __builtin_amdgcn_raw_buffer_load_b32(rx, lane * 20 + 16, 0, 0);
  const int rg2 = 8 + (w & 1);
  unsigned short xr[3];
  xr[0] = __builtin_amdgcn_raw_buffer_load_b16(rx, g8 * 2, (80 * s + 8 * w) * 2, 0);
  xr[1] = __builtin_amdgcn_raw_buffer_load_b16(rx, g8 * 2, (80 * s + 8 * w + 32) * 2, 0);
  xr[2] = __builtin_amdgcn_raw_buffer_load_b16(rx, g8 * 2, (80 * s + 8 * rg2) * 2, 0);
  const int slot = h * 8 + s;
  const int s_off = WS_S + (20 * s + (lane >> 1)) * 1024 + h * 32 + (lane & 1) * 16;
  PIN();
  const rsrc_t rq = mk(p.wq + (size_t)(80 * s) * 12288 + h * 384, 80 * 12288 - h * 384);
  const rsrc_t rg = mk(p.wg + (size_t)(80 * s) * 3072 + h * 96, 80 * 3072 - h * 96);
  const rsrc_t ru = mk(p.wu + (size_t)(80 * s) * 3072 + h * 96, 80 * 3072 - h * 96);
  v4u wa[15];
  {
    const int vq = g8 * 12288 + c8 * 16, vg = c8 < 6 ? g8 * 3072 + c8 * 16 : OOB;
#pragma unroll
    for (int r = 0; r < 3; ++r) {
      const int rgi = r == 0 ? w : r == 1 ? w + 4 : rg2;
      const int mq = vq | ((r == 2 && w >= 2) ? OOB : 0), mg = vg | ((r == 2 && w < 2) ? OOB : 0);
#pragma unroll
      for (int t = 0; t < 3; ++t) wa[r * 5 + t] = LD4(rq, mq + t * 128, rgi * 8 * 12288, NT);
      wa[r * 5 + 3] = LD4(rg, mg, rgi * 8 * 3072, NT);
      wa[r * 5 + 4] = LD4(ru, mg, rgi * 8 * 3072, NT);
    }
  }
  PIN();
  {
    const int cbase = w == 2 ? WS_O + slot * 1024 : WS_Q + slot * 1152 + w * 1024, cn = w == 0 ? 64 : w == 1 ? 8 : w == 2 ? 49 : 40;
    const int co = w == 3 ? s_off : cbase + lane * 16;
    __builtin_amdgcn_raw_buffer_store_b128(v4u{SENT, SENT, SENT, SENT}, rws, lane < cn ? co : OOB, 0, SC1);
  }
  PIN();
  const size_t kvoff = ((size_t)h * SK + (SK - WIN) + 256 * s + 64 * w) * 384;
  const rsrc_t rk = mk(p.k + kvoff, 64 * 384), rv = mk(p.v + kvoff, 64 * 384);
  const int vkv = g8 * 384 + c8 * 16;
  v4u kd[24], vd[24], wc[15];
#define ISSUE_K(lo, hi) do { _Pragma("unroll") for (int i = (lo); i < (hi); ++i) kd[i] = LD4(rk, vkv + (i % 3) * 128, (i / 3) * 3072, NT); PIN(); } while (0)
#define ISSUE_V(lo, hi) do { _Pragma("unroll") for (int i = (lo); i < (hi); ++i) vd[i] = LD4(rv, vkv + (i % 3) * 128, (i / 3) * 3072, NT); PIN(); } while (0)
  STAMP(1);

  v4u mask;
  {
    const unsigned hm = (lane & 1) ? 0xffff0000u : 0x0000ffffu;
    const int dsel = c8 >> 1;
    mask = v4u{dsel == 0 ? hm : 0u, dsel == 1 ? hm : 0u, dsel == 2 ? hm : 0u, dsel == 3 ? hm : 0u};
  }
  v4u xop[3];
  {
    float xv[10] = {lo16(xa.x), hi16(xa.x), lo16(xa.y), hi16(xa.y), lo16(xa.z), hi16(xa.z), lo16(xa.w), hi16(xa.w), lo16(xb), hi16(xb)};
    float sm = 0.f;
#pragma unroll
    for (int i = 0; i < 10; ++i) sm += xv[i];
    const float mean = sum_groups(sum8(sm)) * (1.f / D);
    float sv = 0.f;
#pragma unroll
    for (int i = 0; i < 10; ++i) { const float d = xv[i] - mean; sv = fmaf(d, d, sv); }
    const float var = sum_groups(sum8(sv)) * (1.f / D);
    const float mb = rbf(mean), sd = rbf(__builtin_sqrtf(rbf(rbf(var) + 1e-5f)));
#pragma unroll
    for (int r = 0; r < 3; ++r) xop[r] = diag(rbf(lo16(xr[r]) - mb) / sd, mask);
  }
  PIN();
  ISSUE_K(0, 6);
  f32x4 qa[5];
#pragma unroll
  for (int t = 0; t < 5; ++t) qa[t] = f32x4{0.f, 0.f, 0.f, 0.f};
#pragma unroll
  for (int r = 0; r < 3; ++r) {
#pragma unroll
    for (int t = 0; t < 5; ++t) qa[t] = mfma(xop[r], wa[r * 5 + t], qa[t]);
    PIN();
    if (r < 2) ISSUE_K(6 + 3 * r, 9 + 3 * r);
  }
  const bool dvalid = (lane >> 5) == (g8 & 1);
  const int d4 = 2 * c8 + (g16 & 1), dpart = 2 * w + (lane >> 5);
  if (dvalid) {
#pragma unroll
    for (int t = 0; t < 5; ++t) *(LAS f32x4*)(L + L_RED + (dpart * 80 + t * 16 + d4) * 16) = qa[t];
  }
  asm volatile("s_waitcnt vmcnt(12)" ::: "memory");
  STAMP(2);
  BARRIER();
  if (tid < 74) {
    if (tid < 72) {
      const int src = tid < 60 ? tid : tid + 4;
      f32x4 a = *(LAS f32x4*)(L + L_RED + src * 16);
#pragma unroll
      for (int i = 1; i < 8; ++i) a += *(LAS f32x4*)(L + L_RED + (i * 80 + src) * 16);
      __builtin_amdgcn_raw_buffer_store_b128(v4u{pay(a.x), pay(a.y), pay(a.z), pay(a.w)}, rws, WS_Q + slot * 1152 + tid * 16, 0, SC1);
    } else {
      __builtin_amdgcn_raw_buffer_store_b64(v2u{(unsigned)tag, (unsigned)(tag >> 32)}, rws,
                                            tid == 72 ? WS_HELLO1 + h * 128 + s * 8 : WS_HELLO2 + s * 256 + h * 8, 0, SC1);
    }
  }
  PIN();
  STAMP(3);
  ISSUE_K(12, 18);
  const int hoff = (lane >= 32 && b < 160) ? WS_HELLO2 + (b / 20) * 256 + (lane - 32) * 8 : WS_HELLO1 + h * 128 + c8 * 8;
  v2u hv = __builtin_amdgcn_raw_buffer_load_b64(rws, hoff, 0, SC1);
  PIN();
  ISSUE_K(18, 24);
  ISSUE_V(0, 6);
  if (__builtin_expect(__any(hv.x != (unsigned)tag || hv.y != (unsigned)(tag >> 32)), 0)) {
    for (unsigned spins = 0; spins < SPIN_CAP; ++spins) {
      RELOAD();
      hv = __builtin_amdgcn_raw_buffer_load_b64(rws, hoff, 0, SC1);
      if (!__any(hv.x != (unsigned)tag || hv.y != (unsigned)(tag >> 32))) break;
    }
  }
  RELOAD();
  PIN();
  STAMP(4);
  const int qoff = g8 < 6 ? WS_Q + (h * 8 + c8) * 1152 + (18 * w + g8) * 16 : OOB;
  v4u gq[3];
#pragma unroll
  for (int i = 0; i < 3; ++i) gq[i] = LD4(rws, qoff + i * 96, 0, SC1);
  PIN();
  ISSUE_V(6, 15);
  if (__builtin_expect(__any(umax4(gq[0], umax4(gq[1], umax4(gq[2], 0u))) == SENT), 0)) {
    for (unsigned spins = 0; spins < SPIN_CAP; ++spins) {
      RELOAD();
#pragma unroll
      for (int i = 0; i < 3; ++i) gq[i] = LD4(rws, qoff + i * 96, 0, SC1);
      if (!__any(umax4(gq[0], umax4(gq[1], umax4(gq[2], 0u))) == SENT)) break;
    }
  }
  STAMP(5);
#pragma unroll
  for (int i = 0; i < 3; ++i) {
    const float a0 = sum8(asf(gq[i].x)), a1 = sum8(asf(gq[i].y)), a2 = sum8(asf(gq[i].z)), a3 = sum8(asf(gq[i].w));
    if (c8 == 0 && g8 < 6) *(LAS v2u*)(L + L_Q + (18 * w + 6 * i + g8) * 8) = v2u{pk(a0, a1), pk(a2, a3)};
  }
  BARRIER();
  STAMP(6);
  v4u qf[3];
#pragma unroll
  for (int t = 0; t < 3; ++t) qf[t] = *(LAS v4u*)(L + L_Q + t * 128 + c8 * 16);
  float sc[8];
#pragma unroll
  for (int i = 0; i < 8; ++i) {
    float a = 0.f;
#pragma unroll
    for (int t = 0; t < 3; ++t) {
      a = dot2(kd[3 * i + t].x, qf[t].x, a); a = dot2(kd[3 * i + t].y, qf[t].y, a);
      a = dot2(kd[3 * i + t].z, qf[t].z, a); a = dot2(kd[3 * i + t].w, qf[t].w, a);
    }
    sc[i] = rbf(rbf(sum8(a)) * 0.07216878364870322f);
  }
  float mx = -3.0e38f;
#pragma unroll
  for (int i = 0; i < 8; ++i) mx = fmaxf(mx, sc[i]);
  mx = fmaxf(mx, dpp<ROR8>(mx)); mx = max_x32(max_x16(mx));
  if (lane == 0) *(LAS float*)(L + L_M + w * 4) = mx;
  PIN();
  ISSUE_V(15, 21);
  STAMP(7);
  BARRIER();
  {
    const f32x4 m4 = *(LAS f32x4*)(L + L_M);
    mx = fmaxf(fmaxf(m4.x, m4.y), fmaxf(m4.z, m4.w));
  }
  v4u pop[8];
  float lsum = 0.f;
#pragma unroll
  for (int i = 0; i < 8; ++i) {
    const float e = __builtin_amdgcn_exp2f((sc[i] - mx) * 1.4426950408889634f);
    lsum += e;
    pop[i] = diag(e, mask);
  }
  lsum = sum_groups(lsum);
  if (lane == 0) *(LAS float*)(L + L_M + 16 + w * 4) = lsum;
  PIN();
  ISSUE_V(21, 24);
  STAMP(8);
  const rsrc_t ro = mk(p.wo + (size_t)(192 * h + 48 * w) * 1280 + s * 160, 48 * 1280 - s * 160);
  const rsrc_t rd = mk(p.wd + (size_t)(48 * h + 12 * w) * 1280 + s * 160, 12 * 1280 - s * 160);
  const int vo = c16 < 10 ? g16 * 12 * 1280 + c16 * 16 : OOB, vdn = c16 < 10 ? g16 * 3 * 1280 + c16 * 16 : OOB;
  f32x4 oa[3];
#pragma unroll
  for (int t = 0; t < 3; ++t) oa[t] = f32x4{0.f, 0.f, 0.f, 0.f};
#pragma unroll
  for (int i = 0; i < 8; ++i) {
    if (i >= 3) {
#pragma unroll
      for (int j = 3 * (i - 3); j < 3 * (i - 2); ++j) wc[j] = j < 3 ? LD4(rd, vdn, j * 1280, NT) : LD4(ro, vo, (j - 3) * 1280, NT);
      PIN();
    }
#pragma unroll
    for (int t = 0; t < 3; ++t) oa[t] = mfma(pop[i], vd[3 * i + t], oa[t]);
    PIN();
  }
  if (dvalid) {
#pragma unroll
    for (int t = 0; t < 3; ++t) *(LAS f32x4*)(L + L_RED + (dpart * 48 + t * 16 + d4) * 16) = oa[t];
  }
  STAMP(9);
  BARRIER();
  if (tid < 49) {
    f32x4 a;
    if (tid < 48) {
      a = *(LAS f32x4*)(L + L_RED + tid * 16);
#pragma unroll
      for (int i = 1; i < 8; ++i) a += *(LAS f32x4*)(L + L_RED + (i * 48 + tid) * 16);
    } else {
      const f32x4 l4 = *(LAS f32x4*)(L + L_M + 16);
      a = f32x4{mx, (l4.x + l4.y) + (l4.z + l4.w), 0.f, 0.f};
    }
    __builtin_amdgcn_raw_buffer_store_b128(v4u{pay(a.x), pay(a.y), pay(a.z), pay(a.w)}, rws, WS_O + slot * 1024 + tid * 16, 0, SC1);
  }
  PIN();
  STAMP(10);
  const int ooff = WS_O + (h * 8 + c8) * 1024 + (48 * w + 12 * g16 + 6 * (g8 & 1)) * 4, moff = WS_O + (h * 8 + c8) * 1024 + 768;
  v3u oa0 = __builtin_amdgcn_raw_buffer_load_b96(rws, ooff, 0, SC1), oa1 = __builtin_amdgcn_raw_buffer_load_b96(rws, ooff + 12, 0, SC1);
  v2u ml = __builtin_amdgcn_raw_buffer_load_b64(rws, moff, 0, SC1);
  unsigned short xo = 0;
  if (w == 0) xo = __builtin_amdgcn_raw_buffer_load_b16(rx, b < 160 ? (4 * b + (lane & 3)) * 2 : OOB, 0, 0);
  PIN();
  f32x4 sa = {0.f, 0.f, 0.f, 0.f};
  {
    const LAS unsigned short* gu = (const LAS unsigned short*)(L + L_Q + 384) + 12 * w + 3 * g16;
#pragma unroll
    for (int j = 0; j < 3; ++j) {
      const float hh = lo16(gu[j]) * lo16(gu[48 + j]);
      sa = mfma(diag((g8 & 1) ? hh : 0.f, mask), wc[j], sa);
    }
  }
  PIN();
  for (unsigned spins = 0; spins < SPIN_CAP; ++spins) {
    if (!__any(umax3(umax3(oa0.x, oa0.y, oa0.z), umax3(oa1.x, oa1.y, oa1.z), umax3(ml.x, ml.y, 0u)) == SENT)) break;
    RELOAD();
    oa0 = __builtin_amdgcn_raw_buffer_load_b96(rws, ooff, 0, SC1); oa1 = __builtin_amdgcn_raw_buffer_load_b96(rws, ooff + 12, 0, SC1);
    ml = __builtin_amdgcn_raw_buffer_load_b64(rws, moff, 0, SC1);
  }
  STAMP(11);
  {
    const float M = max8(asf(ml.x));
    const float wt = __builtin_amdgcn_exp2f((asf(ml.x) - M) * 1.4426950408889634f);
    const float f = wt * __builtin_amdgcn_rcpf(sum8(wt * asf(ml.y)));
    float o[12];
    o[0] = sum8(f * asf(oa0.x)); o[1] = sum8(f * asf(oa0.y)); o[2] = sum8(f * asf(oa0.z));
    o[3] = sum8(f * asf(oa1.x)); o[4] = sum8(f * asf(oa1.y)); o[5] = sum8(f * asf(oa1.z));
#pragma unroll
    for (int j = 0; j < 6; ++j) o[6 + j] = dpp<ROR8>(o[j]);
    const v4u mlo = (g8 & 1) ? v4u{0u, 0u, 0u, 0u} : mask;
#pragma unroll
    for (int j = 0; j < 12; ++j) sa = mfma(diag(o[j], mlo), wc[3 + j], sa);
  }
  if (c16 < 10) *(LAS f32x4*)(L + L_RED2 + (w * 40 + (2 * c16 + (g16 & 1)) * 2 + (g16 >> 1)) * 16) = sa;
  STAMP(12);
  BARRIER();
  if (w == 0) {
    if (lane < 40) {
      f32x4 a = *(LAS f32x4*)(L + L_RED2 + lane * 16);
#pragma unroll
      for (int i = 1; i < 4; ++i) a += *(LAS f32x4*)(L + L_RED2 + (i * 40 + lane) * 16);
      __builtin_amdgcn_raw_buffer_store_b128(v4u{pay(a.x), pay(a.y), pay(a.z), pay(a.w)}, rws, s_off, 0, SC1);
    }
    STAMP(13);
    if (b < 160) {
      const int roff = WS_S + b * 1024 + (lane & 31) * 32 + (lane >> 5) * 16;
      v4u r;
      for (unsigned spins = 0; spins < SPIN_CAP; ++spins) {
        RELOAD();
        r = LD4(rws, roff, 0, SC1);
        if (!__any(umax4(r, 0u) == SENT)) break;
      }
      STAMP(14);
      float t[4] = {asf(r.x), asf(r.y), asf(r.z), asf(r.w)};
#pragma unroll
      for (int i = 0; i < 4; ++i) { t[i] = sum8(t[i]); t[i] += dpp<ROR8>(t[i]); t[i] = sum_x16(t[i]); }
      const int c = lane & 3;
      const float mine = c == 0 ? t[0] : c == 1 ? t[1] : c == 2 ? t[2] : t[3];
      const auto sw = __builtin_amdgcn_permlane32_swap(asu(mine), asu(mine), false, false);
      const float sv = rbf(asf(sw[0])), bv = rbf(asf(sw[1]));
      const float gate = rbf(bv / (1.f + __expf(-bv)));
      const unsigned res = pk(lo16(xo) + rbf(sv * gate), 0.f);
      const rsrc_t rout = mk(p.out, D * 2);
      __builtin_amdgcn_raw_buffer_store_b16((unsigned short)res, rout, lane < 4 ? (4 * b + lane) * 2 : OOB, 0, 0);
      if (b == 0 && lane == 0) __builtin_amdgcn_raw_buffer_store_b64(v2u{(unsigned)tag, (unsigned)(tag >> 32)}, rws, WS_EPOCH, 0, SC1);
    }
  }
}
}

extern "C" void kernel_launch(void* const* d_in, const int* in_sizes, int n_in, void* d_out, int out_size, void* d_ws, size_t ws_size,
                              hipStream_t stream) {
  Params p;
  p.x = (const unsigned short*)d_in[0];
  p.k = (const unsigned char*)d_in[1];
  p.v = (const unsigned char*)d_in[2];
  p.wq = (const unsigned char*)d_in[3];
  p.wo = (const unsigned char*)d_in[4];
  p.wg = (const unsigned char*)d_in[6];
  p.wu = (const unsigned char*)d_in[7];
  p.wd = (const unsigned char*)d_in[8];
  p.out = (unsigned short*)d_out;
  p.ws = (unsigned char*)d_ws;
  hipLaunchKernelGGL(fused_decode, dim3(256), dim3(256), 0, stream, p);
}
```

```cpp
#include <hip/hip_runtime.h>
#include <hip/hip_bf16.h>

namespace {
typedef unsigned v4u __attribute__((ext_vector_type(4)));
typedef unsigned v3u __attribute__((ext_vector_type(3)));
typedef unsigned v2u __attribute__((ext_vector_type(2)));
typedef float f32x4 __attribute__((ext_vector_type(4)));
typedef float f32x2 __attribute__((ext_vector_type(2)));
typedef __bf16 bf16x8 __attribute__((ext_vector_type(8)));
typedef __bf16 bf16x2 __attribute__((ext_vector_type(2)));
typedef unsigned long long u64;
typedef __amdgpu_buffer_rsrc_t rsrc_t;
#define LAS __attribute__((address_space(3)))
#define CAS __attribute__((address_space(4)))

constexpr int D = 640, H = 32, DH = 192, SK = 8192, WIN = 2048, F = 1536;
constexpr int NT = 2, SC1 = 16;
constexpr int OOB = 0x40000000;
constexpr unsigned SENT = 0xFFFFFFFFu;
constexpr unsigned SPIN_CAP = 1u << 16;
constexpr int CODE_BYTES = 9600;

constexpr int WS_EPOCH = 0;
constexpr int WS_Q = 4096;
constexpr int WS_O = WS_Q + 256 * 2304;
constexpr int WS_S = WS_O + 256 * 1024;
constexpr int WS_END = WS_S + 160 * 1024;
constexpr int WS_STAMP = 2 << 20;

constexpr int L_RED = 0;
constexpr int L_Q = 10240;
constexpr int L_M = L_Q + 576;
constexpr int L_RED2 = L_M + 64;
constexpr int LDS_BYTES = 88 * 1024;

struct Params {
  const unsigned short* x; const unsigned char* k; const unsigned char* v; const unsigned char* wq; const unsigned char* wo;
  const unsigned char* wg; const unsigned char* wu; const unsigned char* wd; unsigned short* out; unsigned char* ws;
};

__device__ __forceinline__ rsrc_t mk(const void* p, int bytes) { return __builtin_amdgcn_make_buffer_rsrc((void*)p, 0, bytes, 0x00020000); }
__device__ __forceinline__ unsigned pk(float lo, float hi) { bf16x2 v = {(__bf16)lo, (__bf16)hi}; return __builtin_bit_cast(unsigned, v); }
__device__ __forceinline__ float rbf(float a) { return __builtin_bit_cast(float, pk(0.f, a)); }
__device__ __forceinline__ float lo16(unsigned a) { return __builtin_bit_cast(float, a << 16); }
__device__ __forceinline__ float hi16(unsigned a) { return __builtin_bit_cast(float, a & 0xffff0000u); }
__device__ __forceinline__ float asf(unsigned a) { return __builtin_bit_cast(float, a); }
__device__ __forceinline__ unsigned asu(float a) { return __builtin_bit_cast(unsigned, a); }
__device__ __forceinline__ unsigned pay(float a) { const unsigned u = asu(a); return u < SENT - 1u ? u : SENT - 1u; }
template <int CTRL> __device__ __forceinline__ float dpp(float x) {
  return __builtin_bit_cast(float, __builtin_amdgcn_mov_dpp(__builtin_bit_cast(int, x), CTRL, 0xf, 0xf, true));
}
constexpr int XOR1 = 0xB1, XOR2 = 0x4E, XOR7 = 0x141, ROR8 = 0x128;
__device__ __forceinline__ float sum8(float x) { x += dpp<XOR1>(x); x += dpp<XOR2>(x); x += dpp<XOR7>(x); return x; }
__device__ __forceinline__ float max8(float x) { x = fmaxf(x, dpp<XOR1>(x)); x = fmaxf(x, dpp<XOR2>(x)); x = fmaxf(x, dpp<XOR7>(x)); return x; }
__device__ __forceinline__ float sum_x16(float x) { auto s = __builtin_amdgcn_permlane16_swap(asu(x), asu(x), false, false); return asf(s[0]) + asf(s[1]); }
__device__ __forceinline__ float sum_x32(float x) { auto s = __builtin_amdgcn_permlane32_swap(asu(x), asu(x), false, false); return asf(s[0]) + asf(s[1]); }
__device__ __forceinline__ float max_x16(float x) { auto s = __builtin_amdgcn_permlane16_swap(asu(x), asu(x), false, false); return fmaxf(asf(s[0]), asf(s[1])); }
__device__ __forceinline__ float max_x32(float x) { auto s = __builtin_amdgcn_permlane32_swap(asu(x), asu(x), false, false); return fmaxf(asf(s[0]), asf(s[1])); }
__device__ __forceinline__ float sum_groups(float x) { x += dpp<ROR8>(x); return sum_x32(sum_x16(x)); }
__device__ __forceinline__ float dot2(unsigned a, unsigned b, float c) {
  return __builtin_amdgcn_fdot2_f32_bf16(__builtin_bit_cast(bf16x2, a), __builtin_bit_cast(bf16x2, b), c, false);
}
__device__ __forceinline__ f32x4 mfma(v4u a, v4u b, f32x4 c) {
  return __builtin_amdgcn_mfma_f32_16x16x32_bf16(__builtin_bit_cast(bf16x8, a), __builtin_bit_cast(bf16x8, b), c, 0, 0, 0);
}
__device__ __forceinline__ v4u diag(float val, v4u mask) { const unsigned pp = pk(val, val); return v4u{pp & mask.x, pp & mask.y, pp & mask.z, pp & mask.w}; }
__device__ __forceinline__ unsigned umax3(unsigned a, unsigned b, unsigned c) { const unsigned m = a > b ? a : b; return m > c ? m : c; }
__device__ __forceinline__ unsigned umax4(v4u a, unsigned m) { return umax3(a.z, a.w, umax3(a.x, a.y, m)); }
#define RELOAD() asm volatile("" ::: "memory")
#define LD4(rs, vo, so, aux) __builtin_amdgcn_raw_buffer_load_b128(rs, vo, so, aux)
#define PIN() __builtin_amdgcn_sched_barrier(0)
#define BARRIER() do { PIN(); asm volatile("s_waitcnt lgkmcnt(0)\n\ts_barrier" ::: "memory"); PIN(); } while (0)
#define STAMP(i) do { } while (0)

__global__ void __launch_bounds__(320, 2) fused_decode(Params p) {
  __shared__ __attribute__((aligned(16))) unsigned char smem[LDS_BYTES];
  LAS unsigned char* L = (LAS unsigned char*)smem;
  STAMP(0);
  const int b = blockIdx.x, tid = threadIdx.x, lane = tid & 63, w = __builtin_amdgcn_readfirstlane(tid >> 6);
  const int h = 4 * (b & 7) + ((b >> 3) & 3), s = b >> 5;
  const int g8 = lane >> 3, c8 = lane & 7, g16 = lane >> 4, c16 = lane & 15;
  unsigned pf0, pf1;
  {
    const rsrc_t rc = mk((const void*)(__builtin_amdgcn_s_getpc() & ~127ull), CODE_BYTES);
    const int po = (tid ^ 256) * 128;
    pf0 = __builtin_amdgcn_raw_buffer_load_b32(rc, po, 0, 0);
    pf1 = __builtin_amdgcn_raw_buffer_load_b32(rc, po + 8192, 0, 0);
  }
  PIN();
  const int slot = h * 8 + s;
  const rsrc_t rws = mk(p.ws, WS_END), rx = mk(p.x, D * 2);

  if (w == 4) {
    __builtin_amdgcn_s_setprio(3);
    const int s_off = WS_S + (20 * s + (lane >> 1)) * 1024 + h * 32 + (lane & 1) * 16;
    {
      const v4u sent = {SENT, SENT, SENT, SENT};
      __builtin_amdgcn_raw_buffer_store_b128(sent, rws, lane < 49 ? WS_O + slot * 1024 + lane * 16 : OOB, 0, SC1);
      __builtin_amdgcn_raw_buffer_store_b128(sent, rws, lane < 40 ? s_off : OOB, 0, SC1);
    }
    const unsigned short xo = __builtin_amdgcn_raw_buffer_load_b16(rx, (lane & 3) * 2 + b * 8, 0, 0);
    const u64 tag = *(const CAS u64*)(p.ws + WS_EPOCH) + 0x9E3779B97F4A7C15ull;
    const unsigned tlo = (unsigned)tag, thi = (unsigned)(tag >> 32);
    STAMP(1);
    BARRIER();
    STAMP(2);
    {
      f32x2 a[3];
#pragma unroll
      for (int k = 0; k < 3; ++k) {
        const int col = 64 * k + lane, src = col < 120 ? col : col + 8;
        a[k] = *(LAS f32x2*)(L + L_RED + (k == 2 ? src & 159 : src) * 8);
#pragma unroll
        for (int i = 1; i < 8; ++i) a[k] += *(LAS f32x2*)(L + L_RED + i * 1280 + (k == 2 ? src & 159 : src) * 8);
      }
      asm volatile("s_waitcnt vmcnt(0)" :: "v"(pf0), "v"(pf1) : "memory");
#pragma unroll
      for (int k = 0; k < 3; ++k)
        __builtin_amdgcn_raw_buffer_store_b128(v4u{tlo, asu(a[k].x), thi, asu(a[k].y)}, rws,
                                               (k < 2 || lane < 16) ? WS_Q + slot * 2304 + (64 * k + lane) * 16 : OOB, 0, SC1);
    }
    STAMP(3);
    {
      const int q0 = WS_Q + h * 8 * 2304 + lane * 16, q2 = WS_Q + (h * 8 + g16) * 2304 + 2048 + c16 * 16;
      v4u gq[18];
      for (unsigned spins = 0; spins < SPIN_CAP; ++spins) {
        RELOAD();
#pragma unroll
        for (int i = 0; i < 16; ++i) gq[i] = LD4(rws, q0 + (i & 1) * 1024, (i >> 1) * 2304, SC1);
        gq[16] = LD4(rws, q2, 0, SC1); gq[17] = LD4(rws, q2, 4 * 2304, SC1);
        unsigned bad = 0;
#pragma unroll
        for (int i = 0; i < 18; ++i) bad |= (gq[i].x ^ tlo) | (gq[i].z ^ thi);
        if (!__any(bad != 0)) break;
      }
      STAMP(4);
      float a[6] = {0.f, 0.f, 0.f, 0.f, 0.f, 0.f};
#pragma unroll
      for (int i = 0; i < 16; ++i) { a[2 * (i & 1)] += asf(gq[i].y); a[2 * (i & 1) + 1] += asf(gq[i].w); }
      a[4] = sum_x32(sum_x16(asf(gq[16].y) + asf(gq[17].y))); a[5] = sum_x32(sum_x16(asf(gq[16].w) + asf(gq[17].w)));
      *(LAS unsigned*)(L + L_Q + lane * 4) = pk(a[0], a[1]);
      *(LAS unsigned*)(L + L_Q + 256 + lane * 4) = pk(a[2], a[3]);
      if (lane < 16) *(LAS unsigned*)(L + L_Q + 512 + lane * 4) = pk(a[4], a[5]);
    }
    STAMP(5);
    BARRIER();
    if (b < 160) {
      const int hoff = WS_Q + ((lane & 31) * 8 + b / 20) * 2304;
      for (unsigned spins = 0; spins < SPIN_CAP; ++spins) {
        RELOAD();
        const v4u hv = LD4(rws, hoff, 0, SC1);
        if (!__any(hv.x != tlo || hv.z != thi)) break;
      }
    }
    STAMP(6);
    BARRIER();
    float wt[4], mx, lsum;
    {
      const f32x4 m4 = *(LAS f32x4*)(L + L_M), l4 = *(LAS f32x4*)(L + L_M + 16);
      mx = fmaxf(fmaxf(m4.x, m4.y), fmaxf(m4.z, m4.w));
#pragma unroll
      for (int i = 0; i < 4; ++i) wt[i] = __builtin_amdgcn_exp2f((m4[i] - mx) * 1.4426950408889634f);
      lsum = (wt[0] * l4.x + wt[1] * l4.y) + (wt[2] * l4.z + wt[3] * l4.w);
    }
    STAMP(9);
    BARRIER();
    {
      const int src = lane < 48 ? lane : 0;
      f32x4 a = {0.f, 0.f, 0.f, 0.f};
#pragma unroll
      for (int i = 0; i < 4; ++i) a += wt[i] * (*(LAS f32x4*)(L + L_RED + (2 * i * 48 + src) * 16) + *(LAS f32x4*)(L + L_RED + ((2 * i + 1) * 48 + src) * 16));
      if (lane == 48) a = f32x4{mx, lsum, 0.f, 0.f};
      __builtin_amdgcn_raw_buffer_store_b128(v4u{pay(a.x), pay(a.y), pay(a.z), pay(a.w)}, rws, lane < 49 ? WS_O + slot * 1024 + lane * 16 : OOB, 0, SC1);
    }
    STAMP(10);
    BARRIER();
    {
      const int src = lane < 40 ? lane : 0;
      f32x4 a = *(LAS f32x4*)(L + L_RED2 + src * 16);
#pragma unroll
      for (int i = 1; i < 4; ++i) a += *(LAS f32x4*)(L + L_RED2 + (i * 40 + src) * 16);
      __builtin_amdgcn_raw_buffer_store_b128(v4u{pay(a.x), pay(a.y), pay(a.z), pay(a.w)}, rws, lane < 40 ? s_off : OOB, 0, SC1);
    }
    STAMP(13);
    if (b < 160) {
      const int roff = WS_S + b * 1024 + (lane & 31) * 32 + (lane >> 5) * 16;
      v4u r;
      for (unsigned spins = 0; spins < SPIN_CAP; ++spins) {
        RELOAD();
        r = LD4(rws, roff, 0, SC1);
        if (!__any(umax4(r, 0u) == SENT)) break;
      }
      STAMP(14);
      float t[4] = {asf(r.x), asf(r.y), asf(r.z), asf(r.w)};
#pragma unroll
      for (int i = 0; i < 4; ++i) { t[i] = sum8(t[i]); t[i] += dpp<ROR8>(t[i]); t[i] = sum_x16(t[i]); }
      const int c = lane & 3;
      const float mine = c == 0 ? t[0] : c == 1 ? t[1] : c == 2 ? t[2] : t[3];
      const auto sw = __builtin_amdgcn_permlane32_swap(asu(mine), asu(mine), false, false);
      const float sv = rbf(asf(sw[0])), bv = rbf(asf(sw[1]));
      const float gate = rbf(bv / (1.f + __expf(-bv)));
      const unsigned res = pk(lo16(xo) + rbf(sv * gate), 0.f);
      const rsrc_t rout = mk(p.out, D * 2);
      __builtin_amdgcn_raw_buffer_store_b16((unsigned short)res, rout, lane < 4 ? (4 * b + lane) * 2 : OOB, 0, 0);
      if (b == 0 && lane == 0) __builtin_amdgcn_raw_buffer_store_b64(v2u{tlo, thi}, rws, WS_EPOCH, 0, SC1);
    }
  } else {
    const int rg2 = 8 + (w & 1);
    const int vq = (80 * s + 8 * w + g8) * 12288 + h * 384 + c8 * 16, vg = c8 < 6 ? (80 * s + 8 * w + g8) * 3072 + h * 96 + c8 * 16 : OOB;
    const int vq2 = vq + (w >= 2 ? OOB : (rg2 - w) * 8 * 12288), vg2 = vg + (w < 2 ? OOB : (rg2 - w) * 8 * 3072);
    const int vx = (80 * s + 8 * w + g8) * 2;
    const v4u xa = LD4(rx, lane * 20, 0, 0);
    const unsigned xb = __builtin_amdgcn_raw_buffer_load_b32(rx, lane * 20 + 16, 0, 0);
    unsigned short xr[3];
    xr[0] = __builtin_amdgcn_raw_buffer_load_b16(rx, vx, 0, 0);
    xr[1] = __builtin_amdgcn_raw_buffer_load_b16(rx, vx + 64, 0, 0);
    xr[2] = __builtin_amdgcn_raw_buffer_load_b16(rx, vx, (rg2 - w) * 16, 0);
    PIN();
    const rsrc_t rq = mk(p.wq, D * H * DH * 2), rg = mk(p.wg, D * F * 2), ru = mk(p.wu, D * F * 2);
    v4u wa[15];
#pragma unroll
    for (int r = 0; r < 3; ++r) {
#pragma unroll
      for (int t = 0; t < 3; ++t) wa[r * 5 + t] = LD4(rq, (r == 2 ? vq2 : vq) + t * 128, r == 1 ? 32 * 12288 : 0, NT);
      wa[r * 5 + 3] = LD4(rg, r == 2 ? vg2 : vg, r == 1 ? 32 * 3072 : 0, NT);
      wa[r * 5 + 4] = LD4(ru, r == 2 ? vg2 : vg, r == 1 ? 32 * 3072 : 0, NT);
      PIN();
    }
    const rsrc_t rk = mk(p.k, H * SK * DH * 2), rv = mk(p.v, H * SK * DH * 2);
    const int vkv = (h * SK + (SK - WIN) + 256 * s + 64 * w + g8) * 384 + c8 * 16;
    v4u kd[24], vd[24], wc[15];
#define ISSUE_K(lo, hi) do { _Pragma("unroll") for (int i = (lo); i < (hi); ++i) { kd[i] = LD4(rk, vkv + (i % 3) * 128, (i / 3) * 3072, NT); PIN(); } } while (0)
#define ISSUE_V(lo, hi) do { _Pragma("unroll") for (int i = (lo); i < (hi); ++i) { vd[i] = LD4(rv, vkv + (i % 3) * 128, (i / 3) * 3072, NT); PIN(); } } while (0)
    STAMP(1);
    v4u mask;
    {
      const unsigned hm = (lane & 1) ? 0xffff0000u : 0x0000ffffu;
      const int dsel = c8 >> 1;
      mask = v4u{dsel == 0 ? hm : 0u, dsel == 1 ? hm : 0u, dsel == 2 ? hm : 0u, dsel == 3 ? hm : 0u};
    }
    v4u xop[3];
    {
      float xv[10] = {lo16(xa.x), hi16(xa.x), lo16(xa.y), hi16(xa.y), lo16(xa.z), hi16(xa.z), lo16(xa.w), hi16(xa.w), lo16(xb), hi16(xb)};
      float sm = 0.f;
#pragma unroll
      for (int i = 0; i < 10; ++i) sm += xv[i];
      const float mean = sum_groups(sum8(sm)) * (1.f / D);
      float sv = 0.f;
#pragma unroll
      for (int i = 0; i < 10; ++i) { const float d = xv[i] - mean; sv = fmaf(d, d, sv); }
      const float var = sum_groups(sum8(sv)) * (1.f / D);
      const float mb = rbf(mean), sd = rbf(__builtin_sqrtf(rbf(rbf(var) + 1e-5f)));
#pragma unroll
      for (int r = 0; r < 3; ++r) xop[r] = diag(rbf(lo16(xr[r]) - mb) / sd, mask);
    }
    asm volatile("" :: "v"(pf0), "v"(pf1));
    PIN();
    STAMP(16);
    ISSUE_K(0, 6);
    f32x4 qa[5];
#pragma unroll
    for (int t = 0; t < 5; ++t) qa[t] = f32x4{0.f, 0.f, 0.f, 0.f};
#pragma unroll
    for (int r = 0; r < 3; ++r) {
#pragma unroll
      for (int t = 0; t < 5; ++t) qa[t] = mfma(xop[r], wa[r * 5 + t], qa[t]);
      PIN();
      if (r < 2) ISSUE_K(6 + 3 * r, 9 + 3 * r);
    }
    const bool dvalid = (lane >> 5) == (g8 & 1);
    const int d4 = 2 * c8 + (g16 & 1), dpart = 2 * w + (lane >> 5);
    if (dvalid) {
#pragma unroll
      for (int t = 0; t < 5; ++t) *(LAS f32x4*)(L + L_RED + (dpart * 80 + t * 16 + d4) * 16) = qa[t];
    }
    STAMP(2);
    BARRIER();
    STAMP(3);
    ISSUE_K(12, 24);
    STAMP(4);
    ISSUE_V(0, 24);
    STAMP(5);
    BARRIER();
    STAMP(6);
    v4u qf[3];
#pragma unroll
    for (int t = 0; t < 3; ++t) qf[t] = *(LAS v4u*)(L + L_Q + t * 128 + c8 * 16);
    float sc[8];
#pragma unroll
    for (int i = 0; i < 8; ++i) {
      float a = 0.f;
#pragma unroll
      for (int t = 0; t < 3; ++t) {
        a = dot2(kd[3 * i + t].x, qf[t].x, a); a = dot2(kd[3 * i + t].y, qf[t].y, a);
        a = dot2(kd[3 * i + t].z, qf[t].z, a); a = dot2(kd[3 * i + t].w, qf[t].w, a);
      }
      sc[i] = rbf(rbf(sum8(a)) * 0.07216878364870322f);
    }
    PIN();
    STAMP(7);
    const rsrc_t ro = mk(p.wo, H * DH * D * 2), rd = mk(p.wd, F * D * 2);
    const int vo = c16 < 10 ? (192 * h + 48 * w + 12 * g16) * 1280 + s * 160 + c16 * 16 : OOB;
    const int vdn = c16 < 10 ? (48 * h + 12 * w + 3 * g16) * 1280 + s * 160 + c16 * 16 : OOB;
#define ISSUE_C(lo, hi) do { _Pragma("unroll") for (int j = (lo); j < (hi); ++j) { wc[j] = j < 3 ? LD4(rd, vdn, j * 1280, NT) : LD4(ro, vo, (j - 3) * 1280, NT); PIN(); } } while (0)
    ISSUE_C(0, 4);
    float mx = -3.0e38f;
#pragma unroll
    for (int i = 0; i < 8; ++i) mx = fmaxf(mx, sc[i]);
    mx = fmaxf(mx, dpp<ROR8>(mx)); mx = max_x32(max_x16(mx));
    v4u pop[8];
    float lsum = 0.f;
#pragma unroll
    for (int i = 0; i < 8; ++i) {
      const float e = __builtin_amdgcn_exp2f((sc[i] - mx) * 1.4426950408889634f);
      lsum += e;
      pop[i] = diag(e, mask);
    }
    lsum = sum_groups(lsum);
    if (lane == 0) { *(LAS float*)(L + L_M + w * 4) = mx; *(LAS float*)(L + L_M + 16 + w * 4) = lsum; }
    PIN();
    ISSUE_C(4, 7);
    STAMP(8);
    BARRIER();
    f32x4 oa[3];
#pragma unroll
    for (int t = 0; t < 3; ++t) oa[t] = f32x4{0.f, 0.f, 0.f, 0.f};
#pragma unroll
    for (int i = 0; i < 8; ++i) {
      if (i >= 4) ISSUE_C(7 + 2 * (i - 4), 9 + 2 * (i - 4));
#pragma unroll
      for (int t = 0; t < 3; ++t) oa[t] = mfma(pop[i], vd[3 * i + t], oa[t]);
      PIN();
    }
    if (dvalid) {
#pragma unroll
      for (int t = 0; t < 3; ++t) *(LAS f32x4*)(L + L_RED + (dpart * 48 + t * 16 + d4) * 16) = oa[t];
    }
    STAMP(9);
    BARRIER();
    STAMP(10);
    f32x4 sa = {0.f, 0.f, 0.f, 0.f};
    {
      const LAS unsigned short* gu = (const LAS unsigned short*)(L + L_Q + 384) + 12 * w + 3 * g16;
#pragma unroll
      for (int j = 0; j < 3; ++j) {
        const float hh = lo16(gu[j]) * lo16(gu[48 + j]);
        sa = mfma(diag((g8 & 1) ? hh : 0.f, mask), wc[j], sa);
      }
    }
    PIN();
    const int ooff = WS_O + (h * 8 + c8) * 1024 + (48 * w + 12 * g16 + 6 * (g8 & 1)) * 4, moff = WS_O + (h * 8 + c8) * 1024 + 768;
    v3u oa0, oa1; v2u ml;
    for (unsigned spins = 0; spins < SPIN_CAP; ++spins) {
      RELOAD();
      oa0 = __builtin_amdgcn_raw_buffer_load_b96(rws, ooff, 0, SC1); oa1 = __builtin_amdgcn_raw_buffer_load_b96(rws, ooff + 12, 0, SC1);
      ml = __builtin_amdgcn_raw_buffer_load_b64(rws, moff, 0, SC1);
      if (!__any(umax3(umax3(oa0.x, oa0.y, oa0.z), umax3(oa1.x, oa1.y, oa1.z), umax3(ml.x, ml.y, 0u)) == SENT)) break;
    }
    STAMP(11);
    {
      const float M = max8(asf(ml.x));
      const float wt = __builtin_amdgcn_exp2f((asf(ml.x) - M) * 1.4426950408889634f);
      const float f = wt * __builtin_amdgcn_rcpf(sum8(wt * asf(ml.y)));
      float o[12];
      o[0] = sum8(f * asf(oa0.x)); o[1] = sum8(f * asf(oa0.y)); o[2] = sum8(f * asf(oa0.z));
      o[3] = sum8(f * asf(oa1.x)); o[4] = sum8(f * asf(oa1.y)); o[5] = sum8(f * asf(oa1.z));
#pragma unroll
      for (int j = 0; j < 6; ++j) o[6 + j] = dpp<ROR8>(o[j]);
      const v4u mlo = (g8 & 1) ? v4u{0u, 0u, 0u, 0u} : mask;
#pragma unroll
      for (int j = 0; j < 12; ++j) sa = mfma(diag(o[j], mlo), wc[3 + j], sa);
    }
    if (c16 < 10) *(LAS f32x4*)(L + L_RED2 + (w * 40 + (2 * c16 + (g16 & 1)) * 2 + (g16 >> 1)) * 16) = sa;
    STAMP(12);
    BARRIER();
  }
}
}

extern "C" void kernel_launch(void* const* d_in, const int* in_sizes, int n_in, void* d_out, int out_size, void* d_ws, size_t ws_size,
                              hipStream_t stream) {
  Params p;
  p.x = (const unsigned short*)d_in[0];
  p.k = (const unsigned char*)d_in[1];
  p.v = (const unsigned char*)d_in[2];
  p.wq = (const unsigned char*)d_in[3];
  p.wo = (const unsigned char*)d_in[4];
  p.wg = (const unsigned char*)d_in[6];
  p.wu = (const unsigned char*)d_in[7];
  p.wd = (const unsigned char*)d_in[8];
  p.out = (unsigned short*)d_out;
  p.ws = (unsigned char*)d_ws;
  hipLaunchKernelGGL(fused_decode, dim3(256), dim3(320), 0, stream, p);
}
```

```cpp
#include <hip/hip_runtime.h>
#include <hip/hip_bf16.h>
#include <utility>

namespace {
typedef unsigned v4u __attribute__((ext_vector_type(4)));
typedef unsigned v2u __attribute__((ext_vector_type(2)));
typedef float f32x4 __attribute__((ext_vector_type(4)));
typedef float f32x2 __attribute__((ext_vector_type(2)));
typedef __bf16 bf16x8 __attribute__((ext_vector_type(8)));
typedef __bf16 bf16x2 __attribute__((ext_vector_type(2)));
typedef unsigned long long u64;
typedef __amdgpu_buffer_rsrc_t rsrc_t;
#define LAS __attribute__((address_space(3)))
#define CAS __attribute__((address_space(4)))

constexpr int D = 640, H = 32, DH = 192, SK = 8192, WIN = 2048, F = 1536;
constexpr int NT = 2, SC1 = 16;
constexpr int OOB = 0x40000000;
constexpr unsigned SPIN_CAP = 1u << 16;
constexpr int CODE_BYTES = 10496;
#define SW 8
#define NX 22
#define NQ 34
#define BOOST 5
#define BOOST2 3
#define CPRIO 0

constexpr int WS_EPOCH = 0;
constexpr int WS_Q = 4096;
constexpr int WS_O = WS_Q + 256 * 2304;
constexpr int OSTR = 1664;
constexpr int WS_S = WS_O + 256 * OSTR;
constexpr int WS_END = WS_S + 160 * 2048;
constexpr int WS_STAMP = 2 << 20;

constexpr int L_RED = 0;
constexpr int L_Q = 10240;
constexpr int L_M = L_Q + 576;
constexpr int L_TAG = L_M + 32;
constexpr int L_F = L_M + 64;
constexpr int L_RED2 = L_F + 32;
constexpr int LDS_BYTES = 88 * 1024;

struct Params {
  const unsigned short* x; const unsigned char* k; const unsigned char* v; const unsigned char* wq; const unsigned char* wo;
  const unsigned char* wg; const unsigned char* wu; const unsigned char* wd; unsigned short* out; unsigned char* ws;
};

__device__ __forceinline__ rsrc_t mk(const void* p, int bytes) { return __builtin_amdgcn_make_buffer_rsrc((void*)p, 0, bytes, 0x00020000); }
__device__ __forceinline__ unsigned pk(float lo, float hi) { bf16x2 v = {(__bf16)lo, (__bf16)hi}; return __builtin_bit_cast(unsigned, v); }
__device__ __forceinline__ float rbf(float a) { return __builtin_bit_cast(float, pk(0.f, a)); }
__device__ __forceinline__ float lo16(unsigned a) { return __builtin_bit_cast(float, a << 16); }
__device__ __forceinline__ float hi16(unsigned a) { return __builtin_bit_cast(float, a & 0xffff0000u); }
__device__ __forceinline__ float asf(unsigned a) { return __builtin_bit_cast(float, a); }
__device__ __forceinline__ unsigned asu(float a) { return __builtin_bit_cast(unsigned, a); }
template <int CTRL> __device__ __forceinline__ float dpp(float x) {
  return __builtin_bit_cast(float, __builtin_amdgcn_mov_dpp(__builtin_bit_cast(int, x), CTRL, 0xf, 0xf, true));
}
constexpr int XOR1 = 0xB1, XOR2 = 0x4E, XOR7 = 0x141, ROR8 = 0x128, ROR4 = 0x124;
__device__ __forceinline__ float sum8(float x) { x += dpp<XOR1>(x); x += dpp<XOR2>(x); x += dpp<XOR7>(x); return x; }
__device__ __forceinline__ float max8(float x) { x = fmaxf(x, dpp<XOR1>(x)); x = fmaxf(x, dpp<XOR2>(x)); x = fmaxf(x, dpp<XOR7>(x)); return x; }
__device__ __forceinline__ float sum_x16(float x) { auto s = __builtin_amdgcn_permlane16_swap(asu(x), asu(x), false, false); return asf(s[0]) + asf(s[1]); }
__device__ __forceinline__ float sum_x32(float x) { auto s = __builtin_amdgcn_permlane32_swap(asu(x), asu(x), false, false); return asf(s[0]) + asf(s[1]); }
__device__ __forceinline__ float max_x16(float x) { auto s = __builtin_amdgcn_permlane16_swap(asu(x), asu(x), false, false); return fmaxf(asf(s[0]), asf(s[1])); }
__device__ __forceinline__ float max_x32(float x) { auto s = __builtin_amdgcn_permlane32_swap(asu(x), asu(x), false, false); return fmaxf(asf(s[0]), asf(s[1])); }
__device__ __forceinline__ float sum_groups(float x) { x += dpp<ROR8>(x); return sum_x32(sum_x16(x)); }
__device__ __forceinline__ float dot2(unsigned a, unsigned b, float c) {
  return __builtin_amdgcn_fdot2_f32_bf16(__builtin_bit_cast(bf16x2, a), __builtin_bit_cast(bf16x2, b), c, false);
}
__device__ __forceinline__ f32x4 mfma(v4u a, v4u b, f32x4 c) {
  return __builtin_amdgcn_mfma_f32_16x16x32_bf16(__builtin_bit_cast(bf16x8, a), __builtin_bit_cast(bf16x8, b), c, 0, 0, 0);
}
__device__ __forceinline__ v4u diag(float val, v4u mask) { const unsigned pp = pk(val, val); return v4u{pp & mask.x, pp & mask.y, pp & mask.z, pp & mask.w}; }
template <class Fn, int... I> __device__ __forceinline__ void sfor_(Fn&& f, std::integer_sequence<int, I...>) { (f(std::integral_constant<int, I>{}), ...); }
template <int N, class Fn> __device__ __forceinline__ void sfor(Fn&& f) { sfor_(f, std::make_integer_sequence<int, N>{}); }
#define RELOAD() asm volatile("" ::: "memory")
#define LD4(rs, vo, so, aux) __builtin_amdgcn_raw_buffer_load_b128(rs, vo, so, aux)
#define PIN() __builtin_amdgcn_sched_barrier(0)
#define BARRIER() do { PIN(); asm volatile("s_waitcnt lgkmcnt(0)\n\ts_barrier" ::: "memory"); PIN(); } while (0)
#define STAMP(i) do { } while (0)

__global__ void __launch_bounds__(320, 2) fused_decode(Params p) {
  __shared__ __attribute__((aligned(16))) unsigned char smem[LDS_BYTES];
  LAS unsigned char* L = (LAS unsigned char*)smem;
  STAMP(0);
  const int b = blockIdx.x, tid = threadIdx.x, lane = tid & 63, w = __builtin_amdgcn_readfirstlane(tid >> 6);
  const int h = 4 * (b & 7) + ((b >> 3) & 3), s = b >> 5;
  const int g8 = lane >> 3, c8 = lane & 7, g16 = lane >> 4, c16 = lane & 15;
  unsigned pf0, pf1;
  {
    const rsrc_t rc = mk((const void*)(__builtin_amdgcn_s_getpc() & ~127ull), CODE_BYTES);
    const int po = (tid ^ 256) * 128;
    pf0 = __builtin_amdgcn_raw_buffer_load_b32(rc, po, 0, 0);
    pf1 = __builtin_amdgcn_raw_buffer_load_b32(rc, po + 8192, 0, 0);
  }
  PIN();
  constexpr int nch = 10;
  const int c0 = nch * s;

  if (w == 4) {
    __builtin_amdgcn_s_setprio(CPRIO);
    const rsrc_t rws = mk(p.ws, WS_END), rx = mk(p.x, D * 2);
    const unsigned short xo = __builtin_amdgcn_raw_buffer_load_b16(rx, (lane & 3) * 2 + b * 8, 0, 0);
    const u64 tag = *(const CAS u64*)(p.ws + WS_EPOCH) + 0x9E3779B97F4A7C15ull;
    const unsigned tlo = (unsigned)tag, thi = (unsigned)(tag >> 32);
    if (lane == 0) *(LAS v2u*)(L + L_TAG) = v2u{tlo, thi};
    if (lane < 8) *(LAS float*)(L + L_M + lane * 4) = -1.f;
    asm volatile("" :: "v"(pf0), "v"(pf1));
    STAMP(1);
    BARRIER();
    STAMP(2);
    {
      f32x2 a[3];
#pragma unroll
      for (int k = 0; k < 3; ++k) {
        const int col = 64 * k + (k == 2 ? c16 : lane), src = col < 120 ? col : col + 8;
        a[k] = *(LAS f32x2*)(L + L_RED + src * 8);
#pragma unroll
        for (int i = 1; i < 8; ++i) a[k] += *(LAS f32x2*)(L + L_RED + i * 1280 + src * 8);
      }
#pragma unroll
      for (int k = 0; k < 3; ++k)
        __builtin_amdgcn_raw_buffer_store_b128(v4u{tlo, asu(a[k].x), thi, asu(a[k].y)}, rws,
                                               (WS_Q + (h * 8 + s) * 2304 + (64 * k + lane) * 16) | ((k < 2 || lane < 16) ? 0 : OOB), 0, SC1);
    }
    STAMP(3);
    {
      const int q0 = WS_Q + h * 8 * 2304 + lane * 16, q2 = WS_Q + (h * 8 + g16) * 2304 + 2048 + c16 * 16;
      v4u gq[18];
      BARRIER();
      STAMP(17);
      for (unsigned spins = 0; spins < SPIN_CAP; ++spins) {
        RELOAD();
#pragma unroll
        for (int i = 0; i < 16; ++i) gq[i] = LD4(rws, q0 + (i & 1) * 1024, (i >> 1) * 2304, SC1);
        gq[16] = LD4(rws, q2, 0, SC1); gq[17] = LD4(rws, q2, 4 * 2304, SC1);
        unsigned bad = 0;
#pragma unroll
        for (int i = 0; i < 18; ++i) bad |= (gq[i].x ^ tlo) | (gq[i].z ^ thi);
        if (!__any(bad != 0)) break;
      }
      STAMP(4);
      float a[6] = {0.f, 0.f, 0.f, 0.f, 0.f, 0.f};
#pragma unroll
      for (int i = 0; i < 16; ++i) { a[2 * (i & 1)] += asf(gq[i].y); a[2 * (i & 1) + 1] += asf(gq[i].w); }
      a[4] = sum_x32(sum_x16(asf(gq[16].y) + asf(gq[17].y))); a[5] = sum_x32(sum_x16(asf(gq[16].w) + asf(gq[17].w)));
      *(LAS unsigned*)(L + L_Q + lane * 4) = pk(a[0], a[1]);
      *(LAS unsigned*)(L + L_Q + 256 + lane * 4) = pk(a[2], a[3]);
      if (lane < 16) *(LAS unsigned*)(L + L_Q + 512 + lane * 4) = pk(a[4], a[5]);
    }
    STAMP(5);
    BARRIER();
    float wt[4], mx, lsum;
    {
      f32x4 a, c;
      for (unsigned spins = 0; spins < (SPIN_CAP << 4); ++spins) {
        RELOAD();
        a = *(LAS f32x4*)(L + L_M); c = *(LAS f32x4*)(L + L_M + 16);
        if (!__any(a.y < 0.f || a.w < 0.f || c.y < 0.f || c.w < 0.f)) break;
        __builtin_amdgcn_s_sleep(2);
      }
      mx = fmaxf(fmaxf(a.x, a.z), fmaxf(c.x, c.z));
      wt[0] = __builtin_amdgcn_exp2f((a.x - mx) * 1.4426950408889634f); wt[1] = __builtin_amdgcn_exp2f((a.z - mx) * 1.4426950408889634f);
      wt[2] = __builtin_amdgcn_exp2f((c.x - mx) * 1.4426950408889634f); wt[3] = __builtin_amdgcn_exp2f((c.z - mx) * 1.4426950408889634f);
      lsum = (wt[0] * a.y + wt[1] * a.w) + (wt[2] * c.y + wt[3] * c.w);
    }
    const int o_off = WS_O + (h * 8 + s) * OSTR + lane * 16;
    const int s_off = WS_S + (2 * c0 + (lane >> 2)) * 2048 + h * 64 + (lane & 3) * 16;
    const int s_of0 = lane < 8 * nch ? s_off : OOB, s_of1 = lane + 64 < 8 * nch ? s_off + 16 * 2048 : OOB;
    STAMP(9);
    BARRIER();
    {
      f32x2 a = {0.f, 0.f}, c = {0.f, 0.f};
#pragma unroll
      for (int i = 0; i < 4; ++i) {
        a += wt[i] * (*(LAS f32x2*)(L + L_RED + 2 * i * 768 + lane * 8) + *(LAS f32x2*)(L + L_RED + (2 * i + 1) * 768 + lane * 8));
        c += wt[i] * (*(LAS f32x2*)(L + L_RED + 2 * i * 768 + 512 + (lane & 31) * 8) + *(LAS f32x2*)(L + L_RED + (2 * i + 1) * 768 + 512 + (lane & 31) * 8));
      }
      if (lane == 32) c = f32x2{mx, lsum};
      __builtin_amdgcn_raw_buffer_store_b128(v4u{tlo, asu(a.x), thi, asu(a.y)}, rws, o_off, 0, SC1);
      __builtin_amdgcn_raw_buffer_store_b128(v4u{tlo, asu(c.x), thi, asu(c.y)}, rws, lane < 33 ? o_off + 1024 : OOB, 0, SC1);
    }
    STAMP(10);
    {
      const int moff = (WS_O + (h * 8 + c8) * OSTR + 1536) | (lane < 8 ? 0 : OOB);
      v4u ml;
      for (unsigned spins = 0; spins < SPIN_CAP; ++spins) {
        RELOAD();
        ml = LD4(rws, moff, 0, SC1);
        if (!__any(lane < 8 && ((ml.x ^ tlo) | (ml.z ^ thi)) != 0)) break;
      }
      STAMP(11);
      BARRIER();
      const float M = max8(asf(ml.y));
      const float e = __builtin_amdgcn_exp2f((asf(ml.y) - M) * 1.4426950408889634f);
      const float f = e * __builtin_amdgcn_rcpf(sum8(e * asf(ml.w)));
      if (lane < 8) *(LAS float*)(L + L_F + lane * 4) = f;
      BARRIER();
    }
    BARRIER();
    {
      f32x2 a = *(LAS f32x2*)(L + L_RED2 + lane * 8), c = *(LAS f32x2*)(L + L_RED2 + 512 + (lane & 31) * 8);
#pragma unroll
      for (int i = 1; i < 4; ++i) { a += *(LAS f32x2*)(L + L_RED2 + i * 768 + lane * 8); c += *(LAS f32x2*)(L + L_RED2 + i * 768 + 512 + (lane & 31) * 8); }
      __builtin_amdgcn_raw_buffer_store_b128(v4u{tlo, asu(a.x), thi, asu(a.y)}, rws, s_of0, 0, SC1);
      __builtin_amdgcn_raw_buffer_store_b128(v4u{tlo, asu(c.x), thi, asu(c.y)}, rws, s_of1, 0, SC1);
    }
    STAMP(13);
    if (b < 160) {
      const int roff = WS_S + b * 2048 + lane * 16;
      v4u ra, rb;
      for (unsigned spins = 0; spins < SPIN_CAP; ++spins) {
        RELOAD();
        ra = LD4(rws, roff, 0, SC1); rb = LD4(rws, roff + 1024, 0, SC1);
        if (!__any(((ra.x ^ tlo) | (ra.z ^ thi) | (rb.x ^ tlo) | (rb.z ^ thi)) != 0)) break;
      }
      STAMP(14);
      float t[2] = {asf(ra.y) + asf(rb.y), asf(ra.w) + asf(rb.w)};
#pragma unroll
      for (int i = 0; i < 2; ++i) { t[i] += dpp<ROR8>(t[i]); t[i] += dpp<ROR4>(t[i]); t[i] = sum_x32(sum_x16(t[i])); }
      const float s0 = dpp<0x50>(t[0]), s1 = dpp<0x50>(t[1]), b0 = dpp<0xFA>(t[0]), b1 = dpp<0xFA>(t[1]);
      const float sv = rbf((lane & 1) ? s1 : s0), bv = rbf((lane & 1) ? b1 : b0);
      const float gate = rbf(bv / (1.f + __expf(-bv)));
      const unsigned res = pk(lo16(xo) + rbf(sv * gate), 0.f);
      const rsrc_t rout = mk(p.out, D * 2);
      __builtin_amdgcn_raw_buffer_store_b16((unsigned short)res, rout, lane < 4 ? (4 * b + lane) * 2 : OOB, 0, 0);
      if (b == 0 && lane == 0) __builtin_amdgcn_raw_buffer_store_b64(v2u{tlo, thi}, rws, WS_EPOCH, 0, SC1);
    }
  } else {
    const int dg = 8 + (w & 1) - w, r0 = 80 * s + 8 * w + g8;
    int vq = r0 * 12288 + h * 384 + c8 * 16, vg = (r0 * 3072 + h * 96 + c8 * 16) | (c8 < 6 ? 0 : OOB);
    int vx = lane * 20, vr = r0 * 2;
    int vkv = (h * SK + (SK - WIN) + 256 * s + 64 * w + g8) * 384 + c8 * 16;
    asm volatile("" : "+v"(vq), "+v"(vg), "+v"(vx), "+v"(vr), "+v"(vkv));
    PIN();
    const rsrc_t rx = mk(p.x, D * 2);
    STAMP(17);
    const v4u xa = LD4(rx, vx, 0, 0);
    const unsigned xb = __builtin_amdgcn_raw_buffer_load_b32(rx, vx + 16, 0, 0);
    unsigned short xr[3];
    xr[0] = __builtin_amdgcn_raw_buffer_load_b16(rx, vr, 0, 0);
    xr[1] = __builtin_amdgcn_raw_buffer_load_b16(rx, vr + 64, 0, 0);
    xr[2] = __builtin_amdgcn_raw_buffer_load_b16(rx, vr, dg * 16, 0);
    PIN();
    const rsrc_t rq = mk(p.wq, D * H * DH * 2), rg = mk(p.wg, D * F * 2), ru = mk(p.wu, D * F * 2);
    v4u wa[15];
#pragma unroll
    for (int r = 0; r < 3; ++r) {
      const int mq = r == 2 ? vq | (w >= 2 ? OOB : 0) : vq, mg = r == 2 ? vg | (w < 2 ? OOB : 0) : vg, ro_ = r == 2 ? 8 * dg : 32 * r;
#pragma unroll
      for (int t = 0; t < 3; ++t) { wa[r * 5 + t] = LD4(rq, mq + t * 128, ro_ * 12288, NT); PIN(); }
      wa[r * 5 + 3] = LD4(rg, mg, ro_ * 3072, NT); PIN();
      wa[r * 5 + 4] = LD4(ru, mg, ro_ * 3072, NT); PIN();
    }
    const rsrc_t rk = mk(p.k, H * SK * DH * 2), rv = mk(p.v, H * SK * DH * 2);
    const rsrc_t ro = mk(p.wo, H * DH * D * 2), rd = mk(p.wd, F * D * 2);
    const int vo = ((192 * h + 48 * w + 8 * (g16 & 1) + (g16 >> 1)) * 1280 + (c0 + c16) * 16) | (c16 < nch ? 0 : OOB);
    const int vdn = ((48 * h + 12 * w + 3 * g16) * 1280 + (c0 + c16) * 16) | (c16 < nch ? 0 : OOB);
    v4u kd[24], vd[24], wc[15];
    auto issue = [&](auto N) __attribute__((always_inline)) {
      constexpr int n = decltype(N)::value;
      if constexpr (n < 24) kd[n] = LD4(rk, vkv + (n % 3) * 128, (n / 3) * 3072, NT);
      else if constexpr (n < 48) vd[n - 24] = LD4(rv, vkv + (n % 3) * 128, (n / 3 - 8) * 3072, NT);
      else if constexpr (n < 51) wc[n - 48] = LD4(rd, vdn, (n - 48) * 1280, NT);
      else wc[n - 48] = LD4(ro, vo, (16 * ((n - 51) >> 2) + 2 * ((n - 51) & 3)) * 1280, NT);
      PIN();
    };
#define ISSUE(lo, hi, paced) sfor<(hi) - (lo)>([&](auto I_) __attribute__((always_inline)) { \
      if (paced) asm volatile("s_waitcnt vmcnt(%0)" :: "n"(SW - 1) : "memory"); \
      issue(std::integral_constant<int, (lo) + decltype(I_)::value>{}); })
    STAMP(1);
    v4u mask;
    {
      const unsigned hm = (lane & 1) ? 0xffff0000u : 0x0000ffffu;
      const int dsel = c8 >> 1;
      mask = v4u{dsel == 0 ? hm : 0u, dsel == 1 ? hm : 0u, dsel == 2 ? hm : 0u, dsel == 3 ? hm : 0u};
    }
    v4u xop[3];
    {
      float xv[10] = {lo16(xa.x), hi16(xa.x), lo16(xa.y), hi16(xa.y), lo16(xa.z), hi16(xa.z), lo16(xa.w), hi16(xa.w), lo16(xb), hi16(xb)};
      float sm = 0.f;
#pragma unroll
      for (int i = 0; i < 10; ++i) sm += xv[i];
      const float mean = sum_groups(sum8(sm)) * (1.f / D);
      float sv = 0.f;
#pragma unroll
      for (int i = 0; i < 10; ++i) { const float d = xv[i] - mean; sv = fmaf(d, d, sv); }
      const float var = sum_groups(sum8(sv)) * (1.f / D);
      const float mb = rbf(mean), sd = rbf(__builtin_amdgcn_sqrtf(rbf(rbf(var) + 1e-5f))), rs = __builtin_amdgcn_rcpf(sd);
#pragma unroll
      for (int r = 0; r < 3; ++r) { const float d = rbf(lo16(xr[r]) - mb), q = d * rs; xop[r] = diag(fmaf(fmaf(-q, sd, d), rs, q), mask); }
    }
    asm volatile("" :: "v"(pf0), "v"(pf1));
    PIN();
    STAMP(16);
    f32x4 qa[5];
#pragma unroll
    for (int t = 0; t < 5; ++t) qa[t] = f32x4{0.f, 0.f, 0.f, 0.f};
    sfor<15>([&](auto J) __attribute__((always_inline)) {
      constexpr int j = decltype(J)::value;
      qa[j % 5] = mfma(xop[j / 5], wa[j], qa[j % 5]);
      PIN();
      if constexpr (j >= 15 - SW) issue(std::integral_constant<int, j - (15 - SW)>{});
    });
    const bool dvalid = (lane >> 5) == (g8 & 1);
    const int d4 = 2 * c8 + (g16 & 1), dpart = 2 * w + (lane >> 5);
    if (dvalid) {
#pragma unroll
      for (int t = 0; t < 5; ++t) *(LAS f32x4*)(L + L_RED + dpart * 1280 + (t * 16 + d4) * 16) = qa[t];
    }
    STAMP(2);
    BARRIER();
    STAMP(3);
    ISSUE(SW, NX, true);
    STAMP(4);
    BARRIER();
    ISSUE(NX, NQ, true);
    ISSUE(NQ, NQ + BOOST, false);
    STAMP(5);
    BARRIER();
    STAMP(6);
    v4u qf[3];
#pragma unroll
    for (int t = 0; t < 3; ++t) qf[t] = *(LAS v4u*)(L + L_Q + t * 128 + c8 * 16);
    const v2u tg = *(LAS v2u*)(L + L_TAG);
    float sc[8];
#pragma unroll
    for (int i = 0; i < 8; ++i) sc[i] = 0.f;
#pragma unroll
    for (int t = 0; t < 3; ++t)
#pragma unroll
      for (int e = 0; e < 4; ++e) {
#pragma unroll
        for (int i = 0; i < 8; ++i) sc[i] = dot2(kd[3 * i + t][e], qf[t][e], sc[i]);
        PIN();
      }
    STAMP(7);
    ISSUE(NQ + BOOST, NQ + BOOST + BOOST2, false);
#pragma unroll
    for (int i = 0; i < 8; ++i) sc[i] = rbf(rbf(sum8(sc[i])) * 0.07216878364870322f);
    float mx = -3.0e38f;
#pragma unroll
    for (int i = 0; i < 8; ++i) mx = fmaxf(mx, sc[i]);
    mx = fmaxf(mx, dpp<ROR8>(mx)); mx = max_x32(max_x16(mx));
    v4u pop[8];
    float lsum = 0.f;
#pragma unroll
    for (int i = 0; i < 8; ++i) {
      const float e = __builtin_amdgcn_exp2f((sc[i] - mx) * 1.4426950408889634f);
      lsum += e;
      pop[i] = diag(e, mask);
    }
    lsum = sum_groups(lsum);
    if (lane == 0) *(LAS f32x2*)(L + L_M + w * 8) = f32x2{mx, lsum};
    PIN();
    STAMP(8);
    f32x4 oa[3];
#pragma unroll
    for (int t = 0; t < 3; ++t) oa[t] = f32x4{0.f, 0.f, 0.f, 0.f};
    sfor<24>([&](auto J) __attribute__((always_inline)) {
      constexpr int j = decltype(J)::value;
      oa[j % 3] = mfma(pop[j / 3], vd[j], oa[j % 3]);
      PIN();
      if constexpr (24 + j + SW >= NQ + BOOST + BOOST2 && j < 23) issue(std::integral_constant<int, 24 + j + SW>{});
    });
    if (dvalid) {
#pragma unroll
      for (int t = 0; t < 3; ++t) *(LAS f32x4*)(L + L_RED + dpart * 768 + (t * 16 + d4) * 16) = oa[t];
    }
    STAMP(9);
    BARRIER();
    ISSUE(47 + SW, 63, false);
    STAMP(10);
    f32x4 sa = {0.f, 0.f, 0.f, 0.f};
    {
      const LAS unsigned short* gu = (const LAS unsigned short*)(L + L_Q + 384) + 12 * w + 3 * g16;
#pragma unroll
      for (int j = 0; j < 3; ++j) {
        const float hh = lo16(gu[j]) * lo16(gu[48 + j]);
        sa = mfma(diag((g8 & 1) ? hh : 0.f, mask), wc[j], sa);
      }
    }
    PIN();
    const rsrc_t rws = mk(p.ws, WS_END);
    const int ooff = WS_O + (h * 8 + g8) * OSTR + (24 * w + c8) * 16;
    v4u og[3];
    BARRIER();
    STAMP(21);
#pragma unroll
    for (int j = 0; j < 3; ++j) og[j] = LD4(rws, ooff + 128 * j, 0, SC1);
    BARRIER();
    const float f = *(LAS float*)(L + L_F + g8 * 4);
#define OBAD() (((og[0].x ^ tg.x) | (og[0].z ^ tg.y) | (og[1].x ^ tg.x) | (og[1].z ^ tg.y) | (og[2].x ^ tg.x) | (og[2].z ^ tg.y)) != 0)
    if (__builtin_expect(__any(OBAD()), 0)) {
      for (unsigned spins = 0; spins < SPIN_CAP; ++spins) {
        RELOAD();
#pragma unroll
        for (int j = 0; j < 3; ++j) og[j] = LD4(rws, ooff + 128 * j, 0, SC1);
        if (!__any(OBAD())) break;
      }
    }
    STAMP(11);
    {
      const v4u mlo = (g8 & 1) ? v4u{0u, 0u, 0u, 0u} : mask;
#pragma unroll
      for (int j = 0; j < 3; ++j) {
        const auto sw = __builtin_amdgcn_permlane32_swap(asu(f * asf(og[j].y)), asu(f * asf(og[j].w)), false, false);
        float r = sum_x16(asf(sw[0]) + asf(sw[1]));
        r += dpp<ROR8>(r);
        int x = (int)pk(r, r);
        x = __builtin_amdgcn_update_dpp(x, x, ROR4, 0xa, 0xf, false);
        sfor<4>([&](auto N) __attribute__((always_inline)) {
          constexpr int n = decltype(N)::value;
          const unsigned bc = (unsigned)__builtin_amdgcn_mov_dpp(x, 0x150 + n, 0xf, 0xf, true);
          sa = mfma(v4u{bc & mlo.x, bc & mlo.y, bc & mlo.z, bc & mlo.w}, wc[3 + 4 * j + n], sa);
        });
      }
    }
    if (c16 < 12) *(LAS f32x4*)(L + L_RED2 + w * 768 + ((2 * c16 + (g16 & 1)) * 2 + (g16 >> 1)) * 16) = sa;
    STAMP(12);
    BARRIER();
  }
}
}

extern "C" void kernel_launch(void* const* d_in, const int* in_sizes, int n_in, void* d_out, int out_size, void* d_ws, size_t ws_size,
                              hipStream_t stream) {
  Params p;
  p.x = (const unsigned short*)d_in[0];
  p.k = (const unsigned char*)d_in[1];
  p.v = (const unsigned char*)d_in[2];
  p.wq = (const unsigned char*)d_in[3];
  p.wo = (const unsigned char*)d_in[4];
  p.wg = (const unsigned char*)d_in[6];
  p.wu = (const unsigned char*)d_in[7];
  p.wd = (const unsigned char*)d_in[8];
  p.out = (unsigned short*)d_out;
  p.ws = (unsigned char*)d_ws;
  hipLaunchKernelGGL(fused_decode, dim3(256), dim3(320), 0, stream, p);
}
```

```cpp
#include <hip/hip_runtime.h>
#include <hip/hip_bf16.h>
#include <utility>

namespace {
typedef unsigned v4u __attribute__((ext_vector_type(4)));
typedef unsigned v2u __attribute__((ext_vector_type(2)));
typedef unsigned v16u __attribute__((ext_vector_type(16)));
typedef float f32x4 __attribute__((ext_vector_type(4)));
typedef float f32x2 __attribute__((ext_vector_type(2)));
typedef __bf16 bf16x8 __attribute__((ext_vector_type(8)));
typedef __bf16 bf16x2 __attribute__((ext_vector_type(2)));
typedef unsigned long long u64;
typedef __amdgpu_buffer_rsrc_t rsrc_t;
#define LAS __attribute__((address_space(3)))
#define CAS __attribute__((address_space(4)))

constexpr int D = 640, H = 32, DH = 192, SK = 8192, WIN = 2048, F = 1536;
constexpr int NT = 2, SC1 = 16;
constexpr int OOB = 0x40000000;
constexpr unsigned SPIN_CAP = 1u << 16;
constexpr float LOG2E = 1.4426950408889634f;
constexpr int CODE_BYTES = 12416;
#define SW 13
#define NA0 3
#define DA 7
#define TAU 148
#define TAU2 TAU
#define NT2 30
#define TAUC 85
#define TOFF 950
#define NQ 32
#define BOOST 3
#define CTOT 9
#define EARLY 2
#define HOLD 1
#define OWNDRAIN 1
#define FSLEEP 0
#define PSLEEP 0
#define SHINT 0

constexpr int WS_EPOCH = 0;
constexpr int WS_Q = 4096;
constexpr int WS_G = WS_Q + 32 * 5120;
constexpr int WS_O = WS_G + 24 * 5120;
constexpr int WS_M = WS_O + 256 * 1536;
constexpr int WS_B = WS_M + 256 * 128;
constexpr int WS_S = WS_B + 24 * 5120;
constexpr int WS_HQ = WS_S + 160 * 1024;
constexpr int WS_HG = WS_HQ + 32 * 128;
constexpr int WS_HO = WS_HG + 24 * 128;
constexpr int WS_HS = WS_HO + 32 * 128;
constexpr int WS_END = WS_HS + 8 * 128;
constexpr int WS_STAMP = 2 << 20;

constexpr int L_RED = 0;
constexpr int L_X = 6144;
constexpr int L_Q = L_X + 2560;
constexpr int L_H = L_Q + 384;
constexpr int L_M = L_H + 256;
constexpr int L_TAG = L_M + 32;
constexpr int L_F = L_M + 64;
constexpr int L_RB = L_F + 32;
constexpr int L_RS = L_RB + 2048;
constexpr int LDS_BYTES = 88 * 1024;

struct Params {
  const unsigned char* wq; const unsigned char* wg; const unsigned char* wu; const unsigned char* k; const unsigned char* v;
  const unsigned char* wo; const unsigned char* wd; const unsigned short* x; unsigned short* out; unsigned char* ws;
};

__device__ __forceinline__ rsrc_t mk(const void* p, int bytes) { return __builtin_amdgcn_make_buffer_rsrc((void*)p, 0, bytes, 0x00020000); }
__device__ __forceinline__ unsigned pk(float lo, float hi) { bf16x2 v = {(__bf16)lo, (__bf16)hi}; return __builtin_bit_cast(unsigned, v); }
__device__ __forceinline__ float lo16(unsigned a) { return __builtin_bit_cast(float, a << 16); }
__device__ __forceinline__ float hi16(unsigned a) { return __builtin_bit_cast(float, a & 0xffff0000u); }
__device__ __forceinline__ float rbf(float a) { return lo16(pk(a, a)); }
__device__ __forceinline__ float asf(unsigned a) { return __builtin_bit_cast(float, a); }
__device__ __forceinline__ unsigned asu(float a) { return __builtin_bit_cast(unsigned, a); }
template <int CTRL> __device__ __forceinline__ float dpp(float x) {
  return __builtin_bit_cast(float, __builtin_amdgcn_mov_dpp(__builtin_bit_cast(int, x), CTRL, 0xf, 0xf, true));
}
constexpr int XOR1 = 0xB1, XOR2 = 0x4E, XOR7 = 0x141, ROR8 = 0x128, ROR4 = 0x124, BCAST = 0x150;
__device__ __forceinline__ float sum8(float x) { x += dpp<XOR1>(x); x += dpp<XOR2>(x); x += dpp<XOR7>(x); return x; }
__device__ __forceinline__ float max8(float x) { x = fmaxf(x, dpp<XOR1>(x)); x = fmaxf(x, dpp<XOR2>(x)); x = fmaxf(x, dpp<XOR7>(x)); return x; }
__device__ __forceinline__ float sum_x16(float x) { auto s = __builtin_amdgcn_permlane16_swap(asu(x), asu(x), false, false); return asf(s[0]) + asf(s[1]); }
__device__ __forceinline__ float sum_x32(float x) { auto s = __builtin_amdgcn_permlane32_swap(asu(x), asu(x), false, false); return asf(s[0]) + asf(s[1]); }
__device__ __forceinline__ float fold32(float a, float b) { auto s = __builtin_amdgcn_permlane32_swap(asu(a), asu(b), false, false); return asf(s[0]) + asf(s[1]); }
__device__ __forceinline__ float max_x16(float x) { auto s = __builtin_amdgcn_permlane16_swap(asu(x), asu(x), false, false); return fmaxf(asf(s[0]), asf(s[1])); }
__device__ __forceinline__ float max_x32(float x) { auto s = __builtin_amdgcn_permlane32_swap(asu(x), asu(x), false, false); return fmaxf(asf(s[0]), asf(s[1])); }
__device__ __forceinline__ float sum_groups(float x) { x += dpp<ROR8>(x); return sum_x32(sum_x16(x)); }
__device__ __forceinline__ float dot2(unsigned a, unsigned b, float c) {
  return __builtin_amdgcn_fdot2_f32_bf16(__builtin_bit_cast(bf16x2, a), __builtin_bit_cast(bf16x2, b), c, false);
}
__device__ __forceinline__ f32x4 gemv(unsigned pp, v4u mask, v4u b, f32x4 c) {
  const v4u a = {pp & mask.x, pp & mask.y, pp & mask.z, pp & mask.w};
  return __builtin_amdgcn_mfma_f32_16x16x32_bf16(__builtin_bit_cast(bf16x8, a), __builtin_bit_cast(bf16x8, b), c, 0, 0, 0);
}
template <class Fn, int... I> __device__ __forceinline__ void sfor_(Fn&& f, std::integer_sequence<int, I...>) { (f(std::integral_constant<int, I>{}), ...); }
template <int N, class Fn> __device__ __forceinline__ void sfor(Fn&& f) { sfor_(f, std::make_integer_sequence<int, N>{}); }
#define INL __attribute__((always_inline))
#define IC(n) std::integral_constant<int, (n)>{}
#define RELOAD() asm volatile("" ::: "memory")
#define LD4(rs, vo, so, aux) __builtin_amdgcn_raw_buffer_load_b128(rs, vo, so, aux)
#define PUT(a, b, vo) __builtin_amdgcn_raw_buffer_store_b128(v4u{tlo, asu(a), thi, asu(b)}, rws, vo, 0, SC1)
#define HINT(off) __builtin_amdgcn_raw_buffer_store_b32(tlo, rws, lane == 0 ? (off) : OOB, 0, SC1)
#define STALE(g) (((g).x ^ tlo) | ((g).z ^ thi))
#define PIN() __builtin_amdgcn_sched_barrier(0)
#define BARRIER() do { PIN(); asm volatile("s_waitcnt lgkmcnt(0)\n\ts_barrier" ::: "memory"); PIN(); } while (0)
#define STAMP(i) do { } while (0)

template <int N> __device__ __forceinline__ void wait_hint(const unsigned char* ip, unsigned t) {
#pragma nounroll
  for (unsigned spins = 0; spins < 256; ++spins) {
    v16u v;
    asm volatile("s_load_dwordx16 %0, %1, 0x0 glc\n\ts_waitcnt lgkmcnt(0)" : "=&s"(v) : "s"(ip) : "memory");
    unsigned bad = 0;
#pragma unroll
    for (int i = 0; i < N; ++i) bad |= v[i] ^ t;
    if (bad == 0) break;
  }
}

__global__ void __launch_bounds__(320, 2) fused_decode(Params p) {
  __shared__ __attribute__((aligned(16))) unsigned char smem[LDS_BYTES];
  LAS unsigned char* L = (LAS unsigned char*)smem;
  STAMP(0);
  const int b = blockIdx.x, tid = threadIdx.x, lane = tid & 63, w = __builtin_amdgcn_readfirstlane(tid >> 6);
  unsigned pf0, pf1;
  {
    const rsrc_t rc = mk((const void*)(__builtin_amdgcn_s_getpc() & ~127ull), CODE_BYTES);
    const int po = (tid ^ 256) * 128;
    pf0 = __builtin_amdgcn_raw_buffer_load_b32(rc, po, 0, 0);
    pf1 = __builtin_amdgcn_raw_buffer_load_b32(rc, po + 8192, 0, 0);
  }
  PIN();
  const int idx = b >> 3, h = 4 * (b & 7) + (idx & 3), s = idx >> 2;
  const int f3 = (idx * 26) >> 8, cf = 3 * (b & 7) + f3, fj = idx - 10 * f3, fo = idx < 30 ? 0 : OOB;
  const int g8 = lane >> 3, c8 = lane & 7, g16 = lane >> 4, c16 = lane & 15, hi = lane >> 5, l32 = lane & 31;
  const int dpart = 2 * w + (g8 & 1);
  const int R = 240 * s - (s & 4) * 4 + 32 * dpart + 8 * g16, cb = (R >= 640) + (R >= 1280), rq0 = R - 640 * cb;
  const int rf0 = 128 * (fj - 5 * (fj >= 5)) + 32 * w + 4 * g8;
  const bool rok = (unsigned)(R - 240 * s) < 240u;
  int vq = (rq0 * 12288 + h * 384 + cb * 128 + c8 * 16) | (rok ? 0 : OOB), vf = (rf0 * 3072 + cf * 128 + c8 * 16) | fo;
  int vkv = (h * SK + (SK - WIN) + 256 * s + 64 * w + g8) * 384 + c8 * 16;
  int vx = lane * 20;
  asm volatile("" : "+v"(vq), "+v"(vf), "+v"(vkv), "+v"(vx));
  PIN();
  v4u xa = {0u, 0u, 0u, 0u};
  unsigned xb = 0u;
  u64 epoch = 0;
  if (w == 4) {
    __builtin_amdgcn_s_setprio(3);
    const rsrc_t rx = mk(p.x, D * 2);
    xa = LD4(rx, vx, 0, 0);
    xb = __builtin_amdgcn_raw_buffer_load_b32(rx, vx + 16, 0, 0);
    epoch = *(const CAS u64*)(p.ws + WS_EPOCH);
  }
  int role = w;
  asm volatile("" : "+s"(role));

  if (role == 4) {
    const rsrc_t rws = mk(p.ws, WS_END), rx = mk(p.x, D * 2);
    const unsigned short xo = __builtin_amdgcn_raw_buffer_load_b16(rx, (4 * b + 2 * (lane & 1) + hi) * 2, 0, 0);
    {
      float xv[10] = {lo16(xa.x), hi16(xa.x), lo16(xa.y), hi16(xa.y), lo16(xa.z), hi16(xa.z), lo16(xa.w), hi16(xa.w), lo16(xb), hi16(xb)};
      float sm = 0.f;
#pragma unroll
      for (int i = 0; i < 10; ++i) sm += xv[i];
      const float mean = sum_groups(sum8(sm)) * (1.f / D);
      float sv = 0.f;
#pragma unroll
      for (int i = 0; i < 10; ++i) { const float d = xv[i] - mean; sv = fmaf(d, d, sv); }
      const float var = sum_groups(sum8(sv)) * (1.f / D);
      const float mb = rbf(mean), sd = rbf(__builtin_amdgcn_sqrtf(rbf(rbf(var) + 1e-5f))), rs = __builtin_amdgcn_rcpf(sd);
      unsigned xp[10];
#pragma unroll
      for (int i = 0; i < 10; ++i) { const float d = rbf(xv[i] - mb), q = d * rs, r = fmaf(fmaf(-q, sd, d), rs, q); xp[i] = pk(r, r); }
#pragma unroll
      for (int i = 0; i < 5; ++i) *(LAS v2u*)(L + L_X + lane * 40 + i * 8) = v2u{xp[2 * i], xp[2 * i + 1]};
    }
    STAMP(1);
    BARRIER();
    const u64 tag = epoch + 0x9E3779B97F4A7C15ull;
    const unsigned tlo = (unsigned)tag, thi = (unsigned)(tag >> 32);
    if (lane == 0) *(LAS v2u*)(L + L_TAG) = v2u{tlo, thi};
    if (lane < 8) *(LAS float*)(L + L_M + lane * 4) = -1.f;
    asm volatile("" :: "v"(pf0), "v"(pf1));
    const int nd = s == 2 ? 5 : s == 5 ? 3 : 8;
    BARRIER();
    STAMP(2);
    {
      f32x2 a = {0.f, 0.f}, c = {0.f, 0.f};
#pragma unroll
      for (int i = 0; i < 8; ++i) {
        const f32x2 t = *(LAS f32x2*)(L + L_RED + i * 512 + l32 * 8);
        if ((i < nd) == (hi == 0)) a += t;
        c += *(LAS f32x2*)(L + L_RED + i * 512 + 256 + l32 * 8);
      }
      PUT(a.x, a.y, (WS_Q + h * 5120 + (s + (3 * s >> 3)) * 512 + lane * 16) | (hi == 0 || nd < 8 ? 0 : OOB));
      HINT(WS_HQ + h * 128 + s * 4);
      PUT(c.x, c.y, (WS_G + cf * 5120 + fj * 512 + lane * 16) | (hi == 0 ? fo : OOB));
      HINT((WS_HG + cf * 128 + fj * 4) | fo);
    }
    STAMP(3);
    {
      v4u r[5];
      wait_hint<8>(p.ws + WS_HQ + h * 128, tlo);
      STAMP(17);
#pragma nounroll
      for (unsigned spins = 0; spins < SPIN_CAP; ++spins) {
        RELOAD();
#pragma unroll
        for (int i = 0; i < 5; ++i) r[i] = LD4(rws, WS_Q + h * 5120 + lane * 16, i * 1024, SC1);
        if (!__any((STALE(r[0]) | STALE(r[1]) | STALE(r[2]) | STALE(r[3]) | STALE(r[4])) != 0)) break;
      }
      STAMP(4);
      const float a0 = asf(r[0].y) + (hi ? 0.f : asf(r[1].y)), a1 = asf(r[0].w) + (hi ? 0.f : asf(r[1].w));
      const float b0 = asf(r[2].y) + asf(hi ? r[1].y : r[3].y), b1 = asf(r[2].w) + asf(hi ? r[1].w : r[3].w);
      const float c0 = asf(r[4].y) + (hi ? asf(r[3].y) : 0.f), c1 = asf(r[4].w) + (hi ? asf(r[3].w) : 0.f);
      *(LAS unsigned*)(L + L_Q + lane * 4) = pk(fold32(a0, b0), fold32(a1, b1));
      *(LAS unsigned*)(L + L_Q + 256 + l32 * 4) = pk(sum_x32(c0), sum_x32(c1));
    }
    STAMP(5);
    BARRIER();
    if (idx < 30) {
      v4u r[5];
      wait_hint<10>(p.ws + WS_HG + cf * 128, tlo);
#pragma nounroll
      for (unsigned spins = 0; spins < SPIN_CAP; ++spins) {
        RELOAD();
#pragma unroll
        for (int i = 0; i < 5; ++i) r[i] = LD4(rws, WS_G + cf * 5120 + lane * 16, i * 1024, SC1);
        if (!__any((STALE(r[0]) | STALE(r[1]) | STALE(r[2]) | STALE(r[3]) | STALE(r[4])) != 0)) break;
      }
      const float g0 = sum_x32(asf(r[0].y) + asf(r[1].y) + (hi ? 0.f : asf(r[2].y))), g1 = sum_x32(asf(r[0].w) + asf(r[1].w) + (hi ? 0.f : asf(r[2].w)));
      const float u0 = sum_x32(asf(r[3].y) + asf(r[4].y) + (hi ? asf(r[2].y) : 0.f)), u1 = sum_x32(asf(r[3].w) + asf(r[4].w) + (hi ? asf(r[2].w) : 0.f));
      const float h0 = rbf(g0) * rbf(u0), h1 = rbf(g1) * rbf(u1);
      *(LAS v2u*)(L + L_H + l32 * 8) = v2u{pk(h0, h0), pk(h1, h1)};
    } else {
      *(LAS v2u*)(L + L_H + l32 * 8) = v2u{0u, 0u};
    }
    STAMP(6);
    float wt[4];
    {
      f32x4 a, c;
#pragma nounroll
      for (unsigned spins = 0; spins < (SPIN_CAP << 4); ++spins) {
        RELOAD();
        a = *(LAS f32x4*)(L + L_M); c = *(LAS f32x4*)(L + L_M + 16);
        if (!__any(a.y < 0.f || a.w < 0.f || c.y < 0.f || c.w < 0.f)) break;
        __builtin_amdgcn_s_sleep(2);
      }
      const float mx = fmaxf(fmaxf(a.x, a.z), fmaxf(c.x, c.z));
      wt[0] = __builtin_amdgcn_exp2f((a.x - mx) * LOG2E); wt[1] = __builtin_amdgcn_exp2f((a.z - mx) * LOG2E);
      wt[2] = __builtin_amdgcn_exp2f((c.x - mx) * LOG2E); wt[3] = __builtin_amdgcn_exp2f((c.z - mx) * LOG2E);
      PUT(mx, (wt[0] * a.y + wt[1] * a.w) + (wt[2] * c.y + wt[3] * c.w), lane == 0 ? WS_M + (h * 8 + s) * 128 : OOB);
    }
    int o_off = WS_O + (h * 8 + s) * 1536 + lane * 16, o_of1 = (o_off + 1024) | (hi ? OOB : 0);
    int s_off = (WS_S + (20 * s + (lane >> 1)) * 1024 + h * 32 + (lane & 1) * 16) | (lane < 40 ? 0 : OOB);
    asm volatile("" : "+v"(o_off), "+v"(o_of1), "+v"(s_off), "+v"(wt[0]), "+v"(wt[1]), "+v"(wt[2]), "+v"(wt[3]));
    STAMP(8);
    BARRIER();
    HINT(WS_HO + h * 128 + s * 4);
    STAMP(9);
    BARRIER();
    {
      f32x2 a = {0.f, 0.f}, c = {0.f, 0.f};
#pragma unroll
      for (int i = 0; i < 4; ++i) {
        a += wt[i] * (*(LAS f32x2*)(L + L_RED + 2 * i * 768 + lane * 8) + *(LAS f32x2*)(L + L_RED + (2 * i + 1) * 768 + lane * 8));
        c += wt[i] * (*(LAS f32x2*)(L + L_RED + 2 * i * 768 + 512 + l32 * 8) + *(LAS f32x2*)(L + L_RED + (2 * i + 1) * 768 + 512 + l32 * 8));
      }
      PUT(a.x, a.y, o_off);
      PUT(c.x, c.y, o_of1);
    }
    STAMP(10);
    BARRIER();
    {
      f32x2 a = *(LAS f32x2*)(L + L_RB + l32 * 8);
#pragma unroll
      for (int i = 1; i < 8; ++i) a += *(LAS f32x2*)(L + L_RB + i * 256 + l32 * 8);
      PUT(a.x, a.y, (WS_B + cf * 5120 + fj * 512 + lane * 16) | (hi == 0 ? fo : OOB));
    }
    {
      const int moff = (WS_M + (h * 8 + c8) * 128) | (lane < 8 ? 0 : OOB);
      v4u ml = LD4(rws, moff, 0, SC1);
      PIN();
      wait_hint<8>(p.ws + WS_HO + h * 128, tlo);
      if (OWNDRAIN) asm volatile("s_waitcnt vmcnt(2)" ::: "memory");
      STAMP(12);
      BARRIER();
#pragma nounroll
      for (unsigned spins = 0; spins < SPIN_CAP; ++spins) {
        if (!__any(lane < 8 && STALE(ml) != 0)) break;
        RELOAD();
        ml = LD4(rws, moff, 0, SC1);
      }
      const float M = max8(asf(ml.y));
      const float e = __builtin_amdgcn_exp2f((asf(ml.y) - M) * LOG2E);
      const float f = e * __builtin_amdgcn_rcpf(sum8(e * asf(ml.w)));
      if (lane < 8) *(LAS float*)(L + L_F + lane * 4) = f;
      STAMP(11);
      BARRIER();
    }
    BARRIER();
    {
      const int src = lane < 40 ? lane : 0;
      f32x2 a = *(LAS f32x2*)(L + L_RS + src * 8);
#pragma unroll
      for (int i = 1; i < 4; ++i) a += *(LAS f32x2*)(L + L_RS + i * 320 + src * 8);
      PUT(a.x, a.y, s_off);
    }
    STAMP(13);
    if (b < 160) {
      const int boff = (WS_B + (lane >> 1) * 5120 + b * 32 + (lane & 1) * 16) | (lane < 48 ? 0 : OOB);
      v4u r;
#pragma nounroll
      for (unsigned spins = 0; spins < SPIN_CAP; ++spins) {
        RELOAD();
        r = LD4(rws, boff, 0, SC1);
        if (!__any(lane < 48 && STALE(r) != 0)) break;
      }
      float t = fold32(asf(r.y), asf(r.w));
      t += dpp<XOR2>(t); t += dpp<ROR4>(t); t += dpp<ROR8>(t); t = sum_x16(t);
      const float bv = rbf(t);
      float gate = rbf(bv / (1.f + __expf(-bv))), xv = lo16(xo);
      int oo = l32 < 2 ? (4 * b + 2 * (lane & 1) + hi) * 2 : OOB;
      asm volatile("" : "+v"(gate), "+v"(xv), "+v"(oo));
      STAMP(19);
      STAMP(20);
      if (FSLEEP) __builtin_amdgcn_s_sleep(FSLEEP);
#pragma nounroll
      for (unsigned spins = 0; spins < SPIN_CAP; ++spins) {
        RELOAD();
        r = LD4(rws, WS_S + b * 1024 + lane * 16, 0, SC1);
        if (!__any(STALE(r) != 0)) break;
        if (PSLEEP) __builtin_amdgcn_s_sleep(PSLEEP);
      }
      STAMP(14);
      t = fold32(asf(r.y), asf(r.w));
      t += dpp<XOR2>(t); t += dpp<ROR4>(t); t += dpp<ROR8>(t); t = sum_x16(t);
      const float res = xv + rbf(rbf(t) * gate);
      __builtin_amdgcn_raw_buffer_store_b16((unsigned short)pk(res, res), mk(p.out, D * 2), oo, 0, 0);
      if (b == 0) __builtin_amdgcn_raw_buffer_store_b64(v2u{tlo, thi}, rws, lane == 0 ? WS_EPOCH : OOB, 0, SC1);
    }
  } else {
    STAMP(17);
    v4u wa[12];
    u64 t_entry;
    {
      const rsrc_t rq = mk(p.wq, D * H * DH * 2);
#pragma unroll
      for (int i = 0; i < 8; ++i) { wa[i] = LD4(rq, vq, i * 12288, NT); PIN(); }
      asm volatile("s_memrealtime %0" : "=s"(t_entry) :: "memory");
      PIN();
      const rsrc_t rf = mk(fj < 5 ? p.wg : p.wu, D * F * 2);
#pragma unroll
      for (int i = 0; i < 4; ++i) { wa[8 + i] = LD4(rf, vf, i * 3072, NT); PIN(); }
    }
    const rsrc_t rk = mk(p.k, H * SK * DH * 2), rv = mk(p.v, H * SK * DH * 2), ro = mk(p.wo, H * DH * D * 2), rd = mk(p.wd, F * D * 2);
    const int vo = ((192 * h + 48 * w + 8 * (g16 & 1) + (g16 >> 1)) * 1280 + (10 * s + c16) * 16) | (c16 < 10 ? 0 : OOB);
    const int vdn = ((64 * cf + 16 * w + 2 * g8) * 1280 + fj * 128 + c8 * 16) | fo;
    asm volatile("s_waitcnt lgkmcnt(0)" : "+s"(t_entry) :: "memory");
    const unsigned t_start = (unsigned)t_entry;
    v4u kd[24], vd[24], wc[14];
    auto issue = [&](auto N) INL {
      constexpr int n = decltype(N)::value;
      if constexpr (n < 24) kd[n] = LD4(rk, vkv + (n % 3) * 128, (n / 3) * 3072, NT);
      else if constexpr (n < 48) vd[n - 24] = LD4(rv, vkv + (n % 3) * 128, (n / 3 - 8) * 3072, NT);
      else if constexpr (n < 50) wc[n - 48] = LD4(rd, vdn, (n - 48) * 1280, NT);
      else wc[n - 48] = LD4(ro, vo, (16 * ((n - 50) >> 2) + 2 * ((n - 50) & 3)) * 1280, NT);
      PIN();
    };
    auto paced = [&](auto N) INL {
      constexpr int n = decltype(N)::value;
      constexpr unsigned due = (TOFF + (n <= NT2 ? TAU * n : n <= 50 ? TAU * NT2 + TAU2 * (n - NT2) : TAU * NT2 + TAU2 * (50 - NT2) + TAUC * (n - 50))) / 10;
      asm volatile("s_waitcnt vmcnt(%1)\n1:\n\ts_memrealtime vcc\n\ts_waitcnt lgkmcnt(0)\n\ts_sub_u32 vcc_lo, vcc_lo, %0\n\t"
                   "s_cmpk_lt_u32 vcc_lo, %2\n\ts_cbranch_scc1 1b" :: "s"(t_start), "n"(SW - 1), "n"(due) : "vcc", "scc", "memory");
      PIN();
      issue(N);
    };
#define ISSUE(lo, hi, fn) sfor<(hi) - (lo)>([&](auto I_) INL { fn(IC((lo) + decltype(I_)::value)); })
    v4u mask;
    {
      const unsigned hm = (lane & 1) ? 0xffff0000u : 0x0000ffffu;
      const int dsel = c8 >> 1;
      mask = v4u{dsel == 0 ? hm : 0u, dsel == 1 ? hm : 0u, dsel == 2 ? hm : 0u, dsel == 3 ? hm : 0u};
    }
    asm volatile("" :: "v"(pf0), "v"(pf1));
    ISSUE(0, NA0, paced);
    STAMP(1);
    BARRIER();
    v4u xn[3];
    xn[0] = *(LAS v4u*)(L + L_X + (rok ? rq0 : 0) * 4); xn[1] = *(LAS v4u*)(L + L_X + (rok ? rq0 : 0) * 4 + 16); xn[2] = *(LAS v4u*)(L + L_X + rf0 * 4);
    f32x4 qa = {0.f, 0.f, 0.f, 0.f}, fa = {0.f, 0.f, 0.f, 0.f};
    sfor<12>([&](auto J) INL {
      constexpr int j = decltype(J)::value;
      if constexpr (j < 8) qa = gemv(xn[j / 4][j % 4], mask, wa[j], qa); else fa = gemv(xn[2][j % 4], mask, wa[j], fa);
      PIN();
      if constexpr (j >= 11 - DA + NA0 && j < 11) paced(IC(j - (11 - DA)));
    });
    const bool dvalid = hi == (g8 & 1);
    const int d4 = 2 * c8 + (g16 & 1);
    if (dvalid) { *(LAS f32x4*)(L + L_RED + dpart * 512 + d4 * 16) = qa; *(LAS f32x4*)(L + L_RED + dpart * 512 + 256 + d4 * 16) = fa; }
    STAMP(2);
    BARRIER();
    ISSUE(DA, NQ, paced);
    ISSUE(NQ, NQ + BOOST, issue);
    STAMP(5);
    BARRIER();
    STAMP(6);
    constexpr int N1 = NQ + BOOST, N2 = N1 + 7;
    v4u qf[3];
#pragma unroll
    for (int t = 0; t < 3; ++t) qf[t] = *(LAS v4u*)(L + L_Q + t * 128 + c8 * 16);
    const v2u tg = *(LAS v2u*)(L + L_TAG);
    const unsigned tlo = tg.x, thi = tg.y;
    float sc[8];
#pragma unroll
    for (int i = 0; i < 8; ++i) sc[i] = 0.f;
    sfor<3>([&](auto T) INL {
      constexpr int t = decltype(T)::value;
#pragma unroll
      for (int e = 0; e < 4; ++e) {
#pragma unroll
        for (int i = 0; i < 8; ++i) sc[i] = dot2(kd[3 * i + t][e], qf[t][e], sc[i]);
        PIN();
      }
      if constexpr (t > 0) paced(IC(N1 + t - 1));
    });
    sfor<2>([&](auto T) INL {
      constexpr int t = decltype(T)::value;
#pragma unroll
      for (int i = 4 * t; i < 4 * t + 4; ++i) { sc[i] = rbf(rbf(sum8(sc[i])) * 0.07216878364870322f); asm volatile("" : "+v"(sc[i])); }
      PIN();
      paced(IC(N1 + 2 + t));
    });
    float mx = -3.0e38f;
#pragma unroll
    for (int i = 0; i < 8; ++i) mx = fmaxf(mx, sc[i]);
    mx = fmaxf(mx, dpp<ROR8>(mx)); mx = max_x32(max_x16(mx));
    asm volatile("" : "+v"(mx));
    PIN();
    paced(IC(N1 + 4));
    v4u pop[8];
    float lsum = 0.f;
    sfor<2>([&](auto T) INL {
      constexpr int t = decltype(T)::value;
#pragma unroll
      for (int i = 4 * t; i < 4 * t + 4; ++i) {
        const float e = __builtin_amdgcn_exp2f((sc[i] - mx) * LOG2E);
        lsum += e;
        const unsigned pp = pk(e, e);
        pop[i] = v4u{pp & mask.x, pp & mask.y, pp & mask.z, pp & mask.w};
        asm volatile("" : "+v"(pop[i]));
      }
      PIN();
      paced(IC(N1 + 5 + t));
    });
    lsum = sum_groups(lsum);
    if (lane == 0) *(LAS f32x2*)(L + L_M + w * 8) = f32x2{mx, lsum};
    PIN();
    STAMP(8);
    f32x4 oa[3];
#pragma unroll
    for (int t = 0; t < 3; ++t) oa[t] = f32x4{0.f, 0.f, 0.f, 0.f};
    int la = L_RED + dpart * 768 + d4 * 16;
    asm volatile("" : "+v"(la));
    sfor<24>([&](auto J) INL {
      constexpr int j = decltype(J)::value;
      oa[j % 3] = __builtin_amdgcn_mfma_f32_16x16x32_bf16(__builtin_bit_cast(bf16x8, pop[j / 3]), __builtin_bit_cast(bf16x8, vd[j]), oa[j % 3], 0, 0, 0);
      PIN();
      if constexpr (24 + j + SW >= N2 && 24 + j + SW < 48 + CTOT) paced(IC(24 + j + SW));
      if constexpr (j == 23 - EARLY) BARRIER();
    });
    if (dvalid) {
#pragma unroll
      for (int t = 0; t < 3; ++t) *(LAS f32x4*)(L + la + t * 256) = oa[t];
    }
    STAMP(9);
    BARRIER();
    {
      const v2u hh = *(LAS v2u*)(L + L_H + (16 * w + 2 * g8) * 4);
      const f32x4 ba = gemv(hh.y, mask, wc[1], gemv(hh.x, mask, wc[0], f32x4{0.f, 0.f, 0.f, 0.f}));
      if (dvalid) *(LAS f32x4*)(L + L_RB + dpart * 256 + d4 * 16) = ba;
    }
    BARRIER();
    ISSUE(48 + CTOT, 62, paced);
    STAMP(10);
    const rsrc_t rws = mk(p.ws, WS_END);
    int ooff = WS_O + (h * 8 + g8) * 1536 + (24 * w + c8) * 16, ls = L_RS + w * 320 + (2 * c16 + g16) * 16;
    int hoff = w == 0 && lane == 0 ? WS_HS + s * 128 + h * 4 : OOB;
    asm volatile("" : "+v"(ooff), "+v"(ls), "+v"(hoff));
    v4u og[3];
    BARRIER();
    STAMP(21);
#pragma unroll
    for (int j = 0; j < 3; ++j) og[j] = LD4(rws, ooff + 128 * j, 0, SC1);
    BARRIER();
    const float f = *(LAS float*)(L + L_F + g8 * 4);
#define OBAD() ((STALE(og[0]) | STALE(og[1]) | STALE(og[2])) != 0)
    if (__builtin_expect(__any(OBAD()), 0)) {
      STAMP(18);
#pragma nounroll
      for (unsigned spins = 0; spins < SPIN_CAP; ++spins) {
        RELOAD();
        if (OBAD()) {
#pragma unroll
          for (int j = 0; j < 3; ++j) og[j] = LD4(rws, ooff + 128 * j, 0, SC1);
        }
        if (!__any(OBAD())) break;
      }
    }
    STAMP(11);
    {
      int x[3];
#pragma unroll
      for (int j = 0; j < 3; ++j) {
        float r = sum_x16(fold32(f * asf(og[j].y), f * asf(og[j].w)));
        r += dpp<ROR8>(r);
        x[j] = (int)pk(r, r);
        x[j] = __builtin_amdgcn_update_dpp(x[j], x[j], ROR4, 0xa, 0xf, false);
      }
      PIN();
      f32x4 sa = {0.f, 0.f, 0.f, 0.f};
      sfor<12>([&](auto N) INL {
        constexpr int n = decltype(N)::value;
        sa = gemv((unsigned)__builtin_amdgcn_mov_dpp(x[n / 4], BCAST + n % 4, 0xf, 0xf, true), mask, wc[2 + n], sa);
      });
      if (c16 < 10 && hi == 0) *(LAS f32x4*)(L + ls) = sa;
    }
    STAMP(12);
    BARRIER();
  }
}
}

extern "C" void kernel_launch(void* const* d_in, const int* in_sizes, int n_in, void* d_out, int out_size, void* d_ws, size_t ws_size,
                              hipStream_t stream) {
  Params p;
  p.x = (const unsigned short*)d_in[0];
  p.k = (const unsigned char*)d_in[1];
  p.v = (const unsigned char*)d_in[2];
  p.wq = (const unsigned char*)d_in[3];
  p.wo = (const unsigned char*)d_in[4];
  p.wg = (const unsigned char*)d_in[6];
  p.wu = (const unsigned char*)d_in[7];
  p.wd = (const unsigned char*)d_in[8];
  p.out = (unsigned short*)d_out;
  p.ws = (unsigned char*)d_ws;
  hipLaunchKernelGGL(fused_decode, dim3(256), dim3(320), 0, stream, p);
}
```

```cpp
#include <hip/hip_runtime.h>
#include <hip/hip_bf16.h>
#include <utility>

namespace {
typedef unsigned v4u __attribute__((ext_vector_type(4)));
typedef unsigned v2u __attribute__((ext_vector_type(2)));
typedef unsigned v16u __attribute__((ext_vector_type(16)));
typedef float f32x4 __attribute__((ext_vector_type(4)));
typedef float f32x2 __attribute__((ext_vector_type(2)));
typedef __bf16 bf16x8 __attribute__((ext_vector_type(8)));
typedef __bf16 bf16x2 __attribute__((ext_vector_type(2)));
typedef unsigned long long u64;
typedef __amdgpu_buffer_rsrc_t rsrc_t;
#define LAS __attribute__((address_space(3)))
#define CAS __attribute__((address_space(4)))

constexpr int D = 640, H = 32, DH = 192, SK = 8192, WIN = 2048, F = 1536;
constexpr int NT = 2, SC1 = 16;
constexpr int OOB = 0x40000000;
constexpr unsigned SPIN_CAP = 1u << 16;
constexpr float LOG2E = 1.4426950408889634f;
constexpr int CODE_BYTES = 11392;
#define SW 13
#define KPAT 1
#define KS_S 32
#define KS_W 8
#define KSTEP 98304
#define PSTEP 3
#define STAG (h & 3) * 11
#define TFIN 2150
#define BSLEEP 0
#define ROT h
#define LAG 13
#define SWA 13
#define SWC 13
#define NA0 3
#define DA 7
#define TAU 144
#define TAU2 TAU
#define NT2 30
#define TAUC 85
#define TOFF 1330
#define NQ 32
#define BOOST 3
#define CTOT 9
#define EARLY 5
#define HOLD 1
#define OWNDRAIN 1
#define FSLEEP 0
#define PSLEEP 0

constexpr int due_ns(int n) { return TOFF + (n <= NT2 ? TAU * n : n <= 50 ? TAU * NT2 + TAU2 * (n - NT2) : TAU * NT2 + TAU2 * (50 - NT2) + TAUC * (n - 50)); }

constexpr int WS_EPOCH = 0;
constexpr int WS_Q = 4096;
constexpr int WS_G = WS_Q + 32 * 5120;
constexpr int WS_O = WS_G + 24 * 5120;
constexpr int WS_M = WS_O + 256 * 1536;
constexpr int WS_B = WS_M + 256 * 128;
constexpr int WS_S = WS_B + 24 * 5120;
constexpr int WS_HQ = WS_S + 160 * 1024;
constexpr int WS_HG = WS_HQ + 32 * 128;
constexpr int WS_HO = WS_HG + 24 * 128;
constexpr int WS_END = WS_HO + 32 * 128;
constexpr int WS_STAMP = 2 << 20;

constexpr int L_RED = 0;
constexpr int L_X = 6144;
constexpr int L_Q = L_X + 2560;
constexpr int L_H = L_Q + 384;
constexpr int L_M = L_H + 256;
constexpr int L_F = L_M + 64;
constexpr int L_RB = L_F + 32;
constexpr int L_RS = L_RB + 2048;
constexpr int LDS_BYTES = 88 * 1024;

struct Params {
  const unsigned char* wq; const unsigned char* wg; const unsigned char* wu; const unsigned char* k; const unsigned char* v;
  const unsigned char* wo; const unsigned char* wd; const unsigned short* x; unsigned short* out; unsigned char* ws;
};

__device__ __forceinline__ rsrc_t mk(const void* p, int bytes) { return __builtin_amdgcn_make_buffer_rsrc((void*)p, 0, bytes, 0x00020000); }
__device__ __forceinline__ unsigned pk(float lo, float hi) { bf16x2 v = {(__bf16)lo, (__bf16)hi}; return __builtin_bit_cast(unsigned, v); }
__device__ __forceinline__ float lo16(unsigned a) { return __builtin_bit_cast(float, a << 16); }
__device__ __forceinline__ float hi16(unsigned a) { return __builtin_bit_cast(float, a & 0xffff0000u); }
__device__ __forceinline__ float rbf(float a) { return lo16(pk(a, a)); }
__device__ __forceinline__ float asf(unsigned a) { return __builtin_bit_cast(float, a); }
__device__ __forceinline__ unsigned asu(float a) { return __builtin_bit_cast(unsigned, a); }
template <int CTRL> __device__ __forceinline__ float dpp(float x) {
  return __builtin_bit_cast(float, __builtin_amdgcn_mov_dpp(__builtin_bit_cast(int, x), CTRL, 0xf, 0xf, true));
}
constexpr int XOR1 = 0xB1, XOR2 = 0x4E, XOR7 = 0x141, ROR8 = 0x128, ROR4 = 0x124, BCAST = 0x150;
__device__ __forceinline__ float sum8(float x) { x += dpp<XOR1>(x); x += dpp<XOR2>(x); x += dpp<XOR7>(x); return x; }
__device__ __forceinline__ float max8(float x) { x = fmaxf(x, dpp<XOR1>(x)); x = fmaxf(x, dpp<XOR2>(x)); x = fmaxf(x, dpp<XOR7>(x)); return x; }
__device__ __forceinline__ float sum_x16(float x) { auto s = __builtin_amdgcn_permlane16_swap(asu(x), asu(x), false, false); return asf(s[0]) + asf(s[1]); }
__device__ __forceinline__ float sum_x32(float x) { auto s = __builtin_amdgcn_permlane32_swap(asu(x), asu(x), false, false); return asf(s[0]) + asf(s[1]); }
__device__ __forceinline__ float fold32(float a, float b) { auto s = __builtin_amdgcn_permlane32_swap(asu(a), asu(b), false, false); return asf(s[0]) + asf(s[1]); }
__device__ __forceinline__ float max_x16(float x) { auto s = __builtin_amdgcn_permlane16_swap(asu(x), asu(x), false, false); return fmaxf(asf(s[0]), asf(s[1])); }
__device__ __forceinline__ float max_x32(float x) { auto s = __builtin_amdgcn_permlane32_swap(asu(x), asu(x), false, false); return fmaxf(asf(s[0]), asf(s[1])); }
__device__ __forceinline__ float sum_groups(float x) { x += dpp<ROR8>(x); return sum_x32(sum_x16(x)); }
__device__ __forceinline__ float dot2(unsigned a, unsigned b, float c) {
  return __builtin_amdgcn_fdot2_f32_bf16(__builtin_bit_cast(bf16x2, a), __builtin_bit_cast(bf16x2, b), c, false);
}
__device__ __forceinline__ f32x4 gemv(unsigned pp, v4u mask, v4u b, f32x4 c) {
  const v4u a = {pp & mask.x, pp & mask.y, pp & mask.z, pp & mask.w};
  return __builtin_amdgcn_mfma_f32_16x16x32_bf16(__builtin_bit_cast(bf16x8, a), __builtin_bit_cast(bf16x8, b), c, 0, 0, 0);
}
template <class Fn, int... I> __device__ __forceinline__ void sfor_(Fn&& f, std::integer_sequence<int, I...>) { (f(std::integral_constant<int, I>{}), ...); }
template <int N, class Fn> __device__ __forceinline__ void sfor(Fn&& f) { sfor_(f, std::make_integer_sequence<int, N>{}); }
#define INL __attribute__((always_inline))
#define IC(n) std::integral_constant<int, (n)>{}
#define RELOAD() asm volatile("" ::: "memory")
#define LD4(rs, vo, so, aux) __builtin_amdgcn_raw_buffer_load_b128(rs, vo, so, aux)
#define PUT(a, b, vo) __builtin_amdgcn_raw_buffer_store_b128(v4u{tlo, asu(a), thi, asu(b)}, rws, vo, 0, SC1)
#define HINT(off) __builtin_amdgcn_raw_buffer_store_b32(tlo, rws, lane == 0 ? (off) : OOB, 0, SC1)
#define STALE(g) (((g).x ^ tlo) | ((g).z ^ thi))
#define PIN() __builtin_amdgcn_sched_barrier(0)
#define BARRIER() do { PIN(); asm volatile("s_waitcnt lgkmcnt(0)\n\ts_barrier" ::: "memory"); PIN(); } while (0)
#define STAMP(i) do { } while (0)

template <int N> __device__ __forceinline__ void wait_hint(const unsigned char* ip, unsigned t) {
#pragma nounroll
  for (unsigned spins = 0; spins < 256; ++spins) {
    v16u v;
    asm volatile("s_load_dwordx16 %0, %1, 0x0 glc\n\ts_waitcnt lgkmcnt(0)" : "=&s"(v) : "s"(ip) : "memory");
    unsigned bad = 0;
#pragma unroll
    for (int i = 0; i < N; ++i) bad |= v[i] ^ t;
    if (bad == 0) break;
  }
}

__global__ void __launch_bounds__(320, 2) fused_decode(const unsigned char* a_wq, const unsigned char* a_wg, const unsigned char* a_wu, const unsigned short* a_x,
    unsigned char* a_ws, const unsigned char* a_k, const unsigned char* a_v, const unsigned char* a_wo, const unsigned char* a_wd, unsigned short* a_out) {
  __shared__ __attribute__((aligned(16))) unsigned char smem[LDS_BYTES];
  LAS unsigned char* L = (LAS unsigned char*)smem;
  STAMP(0);
  v16u ka; v4u kb;
  asm volatile("s_load_dwordx16 %0, %2, 0x0\n\ts_load_dwordx4 %1, %2, 0x40" : "=&s"(ka), "=&s"(kb) : "s"(__builtin_amdgcn_kernarg_segment_ptr()));
  const int b = blockIdx.x, tid = threadIdx.x, lane = tid & 63, w = __builtin_amdgcn_readfirstlane(tid >> 6);
  unsigned pf0, pf1;
  {
    const rsrc_t rc = mk((const void*)(__builtin_amdgcn_s_getpc() & ~127ull), CODE_BYTES);
    const int po = (tid ^ 256) * 128;
    pf0 = __builtin_amdgcn_raw_buffer_load_b32(rc, po, 0, 0);
    pf1 = __builtin_amdgcn_raw_buffer_load_b32(rc, po + 8192, 0, 0);
  }
  if (w == 4) __builtin_amdgcn_s_setprio(3);
  PIN();
  const int idx = b >> 3, h = 4 * (b & 7) + (idx & 3), s = idx >> 2;
  const int f3 = (idx * 26) >> 8, cf = 3 * (b & 7) + f3, fj = idx - 10 * f3, fo = idx < 30 ? 0 : OOB;
  const int g8 = lane >> 3, c8 = lane & 7, g16 = lane >> 4, c16 = lane & 15, hi = lane >> 5, l32 = lane & 31;
  const int dpart = 2 * w + (g8 & 1);
  const int R = 240 * s - (s & 4) * 4 + 32 * dpart + 8 * g16, cb = (R >= 640) + (R >= 1280), rq0 = R - 640 * cb;
  const int rf0 = 128 * (fj - 5 * (fj >= 5)) + 32 * w + 4 * g8;
  const bool rok = (unsigned)(R - 240 * s) < 240u;
  int vq = (rq0 * 12288 + h * 384 + cb * 128 + c8 * 16) | (rok ? 0 : OOB), vf = (rf0 * 3072 + cf * 128 + c8 * 16) | fo;
  int vkv = (h * SK + (SK - WIN) + KS_S * s + KS_W * w + g8) * 384 + c8 * 16;
  int vx = lane * 20;
  asm volatile("" : "+v"(vq), "+v"(vf), "+v"(vkv), "+v"(vx));
  PIN();
  asm volatile("s_waitcnt lgkmcnt(0)" : "+s"(ka), "+s"(kb) :: "memory");
#define ARG(v, i) (unsigned char*)(((u64)(v)[2 * (i) + 1] << 32) | (v)[2 * (i)])
  const Params p = {ARG(ka, 0), ARG(ka, 1), ARG(ka, 2), ARG(ka, 5), ARG(ka, 6), ARG(ka, 7), ARG(kb, 0), (const unsigned short*)ARG(ka, 3), (unsigned short*)ARG(kb, 1), ARG(ka, 4)};
  v4u xa = {0u, 0u, 0u, 0u};
  unsigned xb = 0u, t_own = 0u;
  if (w == 4) {
    const rsrc_t rx = mk(p.x, D * 2);
    xa = LD4(rx, vx, 0, 0);
    xb = __builtin_amdgcn_raw_buffer_load_b32(rx, vx + 16, 0, 0);
    u64 t;
    asm volatile("s_memrealtime %0\n\ts_waitcnt lgkmcnt(0)" : "=s"(t) :: "memory");
    t_own = (unsigned)t;
  }
  int role = w;
  asm volatile("" : "+s"(role));

  if (role == 4) {
    const rsrc_t rws = mk(p.ws, WS_END), rx = mk(p.x, D * 2);
    const unsigned short xo = __builtin_amdgcn_raw_buffer_load_b16(rx, (4 * b + 2 * (lane & 1) + hi) * 2, 0, 0);
    const u64 tag = *(const CAS u64*)(p.ws + WS_EPOCH) + 0x9E3779B97F4A7C15ull;
    const unsigned tlo = (unsigned)tag, thi = (unsigned)(tag >> 32);
    {
      float xv[10] = {lo16(xa.x), hi16(xa.x), lo16(xa.y), hi16(xa.y), lo16(xa.z), hi16(xa.z), lo16(xa.w), hi16(xa.w), lo16(xb), hi16(xb)};
      float sm = 0.f;
#pragma unroll
      for (int i = 0; i < 10; ++i) sm += xv[i];
      const float mean = sum_groups(sum8(sm)) * (1.f / D);
      float sv = 0.f;
#pragma unroll
      for (int i = 0; i < 10; ++i) { const float d = xv[i] - mean; sv = fmaf(d, d, sv); }
      const float var = sum_groups(sum8(sv)) * (1.f / D);
      const float mb = rbf(mean), sd = rbf(__builtin_amdgcn_sqrtf(rbf(rbf(var) + 1e-5f))), rs = __builtin_amdgcn_rcpf(sd);
      unsigned xp[10];
#pragma unroll
      for (int i = 0; i < 10; ++i) { const float d = rbf(xv[i] - mb), q = d * rs, r = fmaf(fmaf(-q, sd, d), rs, q); xp[i] = pk(r, r); }
#pragma unroll
      for (int i = 0; i < 5; ++i) *(LAS v2u*)(L + L_X + lane * 40 + i * 8) = v2u{xp[2 * i], xp[2 * i + 1]};
    }
    STAMP(1);
    BARRIER();
    if (lane < 8) *(LAS float*)(L + L_M + lane * 4) = -1.f;
    asm volatile("" :: "v"(pf0), "v"(pf1));
    const int nd = s == 2 ? 5 : s == 5 ? 3 : 8;
    int q_off = (WS_Q + h * 5120 + (s + (3 * s >> 3)) * 512 + lane * 16) | (hi == 0 || nd < 8 ? 0 : OOB);
    int g_off = (WS_G + cf * 5120 + fj * 512 + lane * 16) | (hi == 0 ? fo : OOB);
    int hq_off = lane == 0 ? WS_HQ + h * 128 + s * 4 : OOB, hg_off = lane == 0 ? (WS_HG + cf * 128 + fj * 4) | fo : OOB;
    asm volatile("" : "+v"(q_off), "+v"(g_off), "+v"(hq_off), "+v"(hg_off));
    STAMP(7);
    BARRIER();
    STAMP(2);
    {
      f32x2 t[8], c[8];
#pragma unroll
      for (int i = 0; i < 8; ++i) { t[i] = *(LAS f32x2*)(L + L_RED + i * 512 + l32 * 8); c[i] = *(LAS f32x2*)(L + L_RED + i * 512 + 256 + l32 * 8); }
      f32x2 s1 = t[0] + t[1] + t[2], s2 = t[3] + t[4], s3 = t[5] + t[6] + t[7];
      asm volatile("" : "+v"(s1), "+v"(s2), "+v"(s3));
      const f32x2 z = {0.f, 0.f};
      const f32x2 a = (hi ? z : s1) + ((nd == 3) == (hi != 0) ? s2 : z) + ((nd == 8) == (hi == 0) ? s3 : z);
      PUT(a.x, a.y, q_off);
      __builtin_amdgcn_raw_buffer_store_b32(tlo, rws, hq_off, 0, SC1);
      const f32x2 g = ((c[0] + c[1]) + (c[2] + c[3])) + ((c[4] + c[5]) + (c[6] + c[7]));
      PUT(g.x, g.y, g_off);
      __builtin_amdgcn_raw_buffer_store_b32(tlo, rws, hg_off, 0, SC1);
    }
    STAMP(3);
    {
      v4u r[5];
      wait_hint<8>(p.ws + WS_HQ + h * 128, tlo);
      STAMP(17);
#pragma nounroll
      for (unsigned spins = 0; spins < SPIN_CAP; ++spins) {
        RELOAD();
#pragma unroll
        for (int i = 0; i < 5; ++i) r[i] = LD4(rws, WS_Q + h * 5120 + lane * 16, i * 1024, SC1);
        if (!__any((STALE(r[0]) | STALE(r[1]) | STALE(r[2]) | STALE(r[3]) | STALE(r[4])) != 0)) break;
      }
      STAMP(4);
      const float a0 = asf(r[0].y) + (hi ? 0.f : asf(r[1].y)), a1 = asf(r[0].w) + (hi ? 0.f : asf(r[1].w));
      const float b0 = asf(r[2].y) + asf(hi ? r[1].y : r[3].y), b1 = asf(r[2].w) + asf(hi ? r[1].w : r[3].w);
      const float c0 = asf(r[4].y) + (hi ? asf(r[3].y) : 0.f), c1 = asf(r[4].w) + (hi ? asf(r[3].w) : 0.f);
      *(LAS unsigned*)(L + L_Q + lane * 4) = pk(fold32(a0, b0), fold32(a1, b1));
      *(LAS unsigned*)(L + L_Q + 256 + l32 * 4) = pk(sum_x32(c0), sum_x32(c1));
    }
    STAMP(5);
    BARRIER();
    if (idx < 30) {
      v4u r[5];
      wait_hint<10>(p.ws + WS_HG + cf * 128, tlo);
#pragma nounroll
      for (unsigned spins = 0; spins < SPIN_CAP; ++spins) {
        RELOAD();
#pragma unroll
        for (int i = 0; i < 5; ++i) r[i] = LD4(rws, WS_G + cf * 5120 + lane * 16, i * 1024, SC1);
        if (!__any((STALE(r[0]) | STALE(r[1]) | STALE(r[2]) | STALE(r[3]) | STALE(r[4])) != 0)) break;
      }
      const float g0 = sum_x32(asf(r[0].y) + asf(r[1].y) + (hi ? 0.f : asf(r[2].y))), g1 = sum_x32(asf(r[0].w) + asf(r[1].w) + (hi ? 0.f : asf(r[2].w)));
      const float u0 = sum_x32(asf(r[3].y) + asf(r[4].y) + (hi ? asf(r[2].y) : 0.f)), u1 = sum_x32(asf(r[3].w) + asf(r[4].w) + (hi ? asf(r[2].w) : 0.f));
      const float h0 = rbf(g0) * rbf(u0), h1 = rbf(g1) * rbf(u1);
      *(LAS v2u*)(L + L_H + l32 * 8) = v2u{pk(h0, h0), pk(h1, h1)};
    } else {
      *(LAS v2u*)(L + L_H + l32 * 8) = v2u{0u, 0u};
    }
    STAMP(6);
    float wt[4];
    {
      f32x4 a, c;
#pragma nounroll
      for (unsigned spins = 0; spins < (SPIN_CAP << 4); ++spins) {
        RELOAD();
        a = *(LAS f32x4*)(L + L_M); c = *(LAS f32x4*)(L + L_M + 16);
        if (!__any(a.y < 0.f || a.w < 0.f || c.y < 0.f || c.w < 0.f)) break;
        __builtin_amdgcn_s_sleep(2);
      }
      const float mx = fmaxf(fmaxf(a.x, a.z), fmaxf(c.x, c.z));
      wt[0] = __builtin_amdgcn_exp2f((a.x - mx) * LOG2E); wt[1] = __builtin_amdgcn_exp2f((a.z - mx) * LOG2E);
      wt[2] = __builtin_amdgcn_exp2f((c.x - mx) * LOG2E); wt[3] = __builtin_amdgcn_exp2f((c.z - mx) * LOG2E);
      PUT(mx, (wt[0] * a.y + wt[1] * a.w) + (wt[2] * c.y + wt[3] * c.w), lane == 0 ? WS_M + (h * 8 + s) * 128 : OOB);
    }
    int o_off = WS_O + (h * 8 + s) * 1536 + lane * 16, o_of1 = (o_off + 1024) | (hi ? OOB : 0);
    asm volatile("" : "+v"(o_off), "+v"(o_of1), "+v"(wt[0]), "+v"(wt[1]), "+v"(wt[2]), "+v"(wt[3]));
    STAMP(8);
    BARRIER();
    HINT(WS_HO + h * 128 + s * 4);
    STAMP(9);
    BARRIER();
    {
      f32x2 a = {0.f, 0.f}, c = {0.f, 0.f};
#pragma unroll
      for (int i = 0; i < 4; ++i) {
        a += wt[i] * (*(LAS f32x2*)(L + L_RED + 2 * i * 768 + lane * 8) + *(LAS f32x2*)(L + L_RED + (2 * i + 1) * 768 + lane * 8));
        c += wt[i] * (*(LAS f32x2*)(L + L_RED + 2 * i * 768 + 512 + l32 * 8) + *(LAS f32x2*)(L + L_RED + (2 * i + 1) * 768 + 512 + l32 * 8));
      }
      PUT(a.x, a.y, o_off);
      PUT(c.x, c.y, o_of1);
    }
    STAMP(10);
    BARRIER();
    {
      f32x2 a = *(LAS f32x2*)(L + L_RB + l32 * 8);
#pragma unroll
      for (int i = 1; i < 8; ++i) a += *(LAS f32x2*)(L + L_RB + i * 256 + l32 * 8);
      PUT(a.x, a.y, (WS_B + cf * 5120 + fj * 512 + lane * 16) | (hi == 0 ? fo : OOB));
    }
    {
      const int moff = (WS_M + (h * 8 + c8) * 128) | (lane < 8 ? 0 : OOB);
      v4u ml = LD4(rws, moff, 0, SC1);
      PIN();
      STAMP(16);
      wait_hint<8>(p.ws + WS_HO + h * 128, tlo);
      STAMP(22);
      if (OWNDRAIN) asm volatile("s_waitcnt vmcnt(2)" ::: "memory");
      STAMP(12);
      BARRIER();
#pragma nounroll
      for (unsigned spins = 0; spins < SPIN_CAP; ++spins) {
        if (!__any(lane < 8 && STALE(ml) != 0)) break;
        RELOAD();
        ml = LD4(rws, moff, 0, SC1);
      }
      const float M = max8(asf(ml.y));
      const float e = __builtin_amdgcn_exp2f((asf(ml.y) - M) * LOG2E);
      const float f = e * __builtin_amdgcn_rcpf(sum8(e * asf(ml.w)));
      if (lane < 8) *(LAS float*)(L + L_F + lane * 4) = f;
      STAMP(11);
      BARRIER();
    }
    const int boff = (WS_B + (lane >> 1) * 5120 + b * 32 + (lane & 1) * 16) | (lane < 48 && b < 160 ? 0 : OOB);
    if (BSLEEP) __builtin_amdgcn_s_sleep(BSLEEP);
    v4u r = LD4(rws, boff, 0, SC1);
    BARRIER();
    if (b < 160) {
#pragma nounroll
      for (unsigned spins = 0; spins < SPIN_CAP; ++spins) {
        if (!__any(lane < 48 && STALE(r) != 0)) break;
        RELOAD();
        r = LD4(rws, boff, 0, SC1);
      }
      float t = fold32(asf(r.y), asf(r.w));
      t += dpp<XOR2>(t); t += dpp<ROR4>(t); t += dpp<ROR8>(t); t = sum_x16(t);
      const float bv = rbf(t);
      float gate = rbf(bv / (1.f + __expf(-bv))), xv = lo16(xo);
      int oo = l32 < 2 ? (4 * b + 2 * (lane & 1) + hi) * 2 : OOB;
      asm volatile("" : "+v"(gate), "+v"(xv), "+v"(oo));
      STAMP(19);
      if (FSLEEP) __builtin_amdgcn_s_sleep(FSLEEP);
      asm volatile("1:\n\ts_memrealtime vcc\n\ts_waitcnt lgkmcnt(0)\n\ts_sub_u32 vcc_lo, vcc_lo, %0\n\t"
                   "s_cmpk_lt_u32 vcc_lo, %1\n\ts_cbranch_scc1 1b" :: "s"(t_own), "n"((due_ns(61) + TFIN) / 10) : "vcc", "scc", "memory");
#pragma nounroll
      for (unsigned spins = 0; spins < SPIN_CAP; ++spins) {
        RELOAD();
        r = LD4(rws, WS_S + b * 1024 + lane * 16, 0, SC1);
        if (!__any(STALE(r) != 0)) break;
        if (PSLEEP) __builtin_amdgcn_s_sleep(PSLEEP);
      }
      STAMP(14);
      t = fold32(asf(r.y), asf(r.w));
      t += dpp<XOR2>(t); t += dpp<ROR4>(t); t += dpp<ROR8>(t); t = sum_x16(t);
      const float res = xv + rbf(rbf(t) * gate);
      __builtin_amdgcn_raw_buffer_store_b16((unsigned short)pk(res, res), mk(p.out, D * 2), oo, 0, 0);
      if (b == 0) __builtin_amdgcn_raw_buffer_store_b64(v2u{tlo, thi}, rws, lane == 0 ? WS_EPOCH : OOB, 0, SC1);
    }
  } else {
    STAMP(17);
    v4u wa[12];
    u64 epoch, t_entry;
    asm volatile("s_memrealtime %0\n\ts_load_dwordx2 %1, %2, 0x0" : "=&s"(t_entry), "=&s"(epoch) : "s"(p.ws + WS_EPOCH) : "memory");
    PIN();
    {
      const rsrc_t rq = mk(p.wq, D * H * DH * 2);
#pragma unroll
      for (int i = 0; i < 8; ++i) { wa[i] = LD4(rq, vq, i * 12288, NT); PIN(); }
      const rsrc_t rf = mk(fj < 5 ? p.wg : p.wu, D * F * 2);
#pragma unroll
      for (int i = 0; i < 4; ++i) { wa[8 + i] = LD4(rf, vf, i * 3072, NT); PIN(); }
    }
    const rsrc_t rk = mk(p.k, H * SK * DH * 2), rv = mk(p.v, H * SK * DH * 2), ro = mk(p.wo, H * DH * D * 2), rd = mk(p.wd, F * D * 2);
    const int vo = ((192 * h + 48 * w + 8 * (g16 & 1) + (g16 >> 1)) * 1280 + (10 * s + c16) * 16) | (c16 < 10 ? 0 : OOB);
    const int vdn = ((64 * cf + 16 * w + 2 * g8) * 1280 + fj * 128 + c8 * 16) | fo;
    asm volatile("s_waitcnt lgkmcnt(0)" : "+s"(t_entry), "+s"(epoch) :: "memory");
    const unsigned t_start = (unsigned)t_entry + (STAG);
    const u64 tag = epoch + 0x9E3779B97F4A7C15ull;
    const unsigned tlo = (unsigned)tag, thi = (unsigned)(tag >> 32);
    const int rot = ROT;
    v4u kd[24], vd[24], wc[14];
    auto issue = [&](auto N) INL {
      constexpr int n = decltype(N)::value;
      if constexpr (n < 24) kd[n] = LD4(rk, vkv + (n % 3) * 128, ((n / 3 + rot) & 7) * KSTEP, NT);
      else if constexpr (n < 48) vd[n - 24] = LD4(rv, vkv + (n % 3) * 128, ((n / 3 + rot) & 7) * KSTEP, NT);
      else if constexpr (n < 50) wc[n - 48] = LD4(rd, vdn, (n - 48) * 1280, NT);
      else wc[n - 48] = LD4(ro, vo, (16 * ((n - 50) >> 2) + 2 * ((n - 50) & 3)) * 1280, NT);
      PIN();
    };
    auto paced = [&](auto N) INL {
      constexpr int n = decltype(N)::value;
      constexpr unsigned due = due_ns(n) / 10;
      if constexpr (n >= 48 || n % PSTEP == 0)
      asm volatile("s_waitcnt vmcnt(%1)\n1:\n\ts_memrealtime vcc\n\ts_waitcnt lgkmcnt(0)\n\ts_sub_u32 vcc_lo, vcc_lo, %0\n\t"
                   "s_cmpk_lt_u32 vcc_lo, %2\n\ts_cbranch_scc1 1b" :: "s"(t_start), "n"((n < NA0 + 4 ? SWA : n < 48 ? SW : SWC) - (n < 48 ? PSTEP : 1)), "n"(due) : "vcc", "scc", "memory");
      PIN();
      issue(N);
    };
#define ISSUE(lo, hi, fn) sfor<(hi) - (lo)>([&](auto I_) INL { fn(IC((lo) + decltype(I_)::value)); })
    v4u mask;
    {
      const unsigned hm = (lane & 1) ? 0xffff0000u : 0x0000ffffu;
      const int dsel = c8 >> 1;
      mask = v4u{dsel == 0 ? hm : 0u, dsel == 1 ? hm : 0u, dsel == 2 ? hm : 0u, dsel == 3 ? hm : 0u};
    }
    asm volatile("" :: "v"(pf0), "v"(pf1));
    ISSUE(0, NA0, paced);
    STAMP(1);
    BARRIER();
    v4u xn[3];
    xn[0] = *(LAS v4u*)(L + L_X + (rok ? rq0 : 0) * 4); xn[1] = *(LAS v4u*)(L + L_X + (rok ? rq0 : 0) * 4 + 16); xn[2] = *(LAS v4u*)(L + L_X + rf0 * 4);
    f32x4 qa = {0.f, 0.f, 0.f, 0.f}, fa = {0.f, 0.f, 0.f, 0.f};
    sfor<12>([&](auto J) INL {
      constexpr int j = decltype(J)::value;
      if constexpr (j < 8) qa = gemv(xn[j / 4][j % 4], mask, wa[j], qa); else fa = gemv(xn[2][j % 4], mask, wa[j], fa);
      PIN();
      if constexpr (j >= 11 - DA + NA0 && j < 11) paced(IC(j - (11 - DA)));
    });
    const bool dvalid = hi == (g8 & 1);
    const int d4 = 2 * c8 + (g16 & 1);
    if (dvalid) { *(LAS f32x4*)(L + L_RED + dpart * 512 + d4 * 16) = qa; *(LAS f32x4*)(L + L_RED + dpart * 512 + 256 + d4 * 16) = fa; }
    STAMP(2);
    BARRIER();
    ISSUE(DA, NQ, paced);
    ISSUE(NQ, NQ + BOOST, issue);
    STAMP(5);
    BARRIER();
    STAMP(6);
    constexpr int N1 = NQ + BOOST, N2 = N1 + 7;
    v4u qf[3];
#pragma unroll
    for (int t = 0; t < 3; ++t) qf[t] = *(LAS v4u*)(L + L_Q + t * 128 + c8 * 16);
    float sc[8];
#pragma unroll
    for (int i = 0; i < 8; ++i) sc[i] = 0.f;
    sfor<3>([&](auto T) INL {
      constexpr int t = decltype(T)::value;
#pragma unroll
      for (int e = 0; e < 4; ++e) {
#pragma unroll
        for (int i = 0; i < 8; ++i) sc[i] = dot2(kd[3 * i + t][e], qf[t][e], sc[i]);
        PIN();
      }
      if constexpr (t > 0) paced(IC(N1 + t - 1));
    });
    sfor<2>([&](auto T) INL {
      constexpr int t = decltype(T)::value;
#pragma unroll
      for (int i = 4 * t; i < 4 * t + 4; ++i) { sc[i] = rbf(rbf(sum8(sc[i])) * 0.07216878364870322f); asm volatile("" : "+v"(sc[i])); }
      PIN();
      paced(IC(N1 + 2 + t));
    });
    float mx = -3.0e38f;
#pragma unroll
    for (int i = 0; i < 8; ++i) mx = fmaxf(mx, sc[i]);
    mx = fmaxf(mx, dpp<ROR8>(mx)); mx = max_x32(max_x16(mx));
    asm volatile("" : "+v"(mx));
    PIN();
    paced(IC(N1 + 4));
    v4u pop[8];
    float lsum = 0.f;
    sfor<2>([&](auto T) INL {
      constexpr int t = decltype(T)::value;
#pragma unroll
      for (int i = 4 * t; i < 4 * t + 4; ++i) {
        const float e = __builtin_amdgcn_exp2f((sc[i] - mx) * LOG2E);
        lsum += e;
        const unsigned pp = pk(e, e);
        pop[i] = v4u{pp & mask.x, pp & mask.y, pp & mask.z, pp & mask.w};
        asm volatile("" : "+v"(pop[i]));
      }
      PIN();
      paced(IC(N1 + 5 + t));
    });
    lsum = sum_groups(lsum);
    if (lane == 0) *(LAS f32x2*)(L + L_M + w * 8) = f32x2{mx, lsum};
    PIN();
    STAMP(8);
    f32x4 oa[3];
#pragma unroll
    for (int t = 0; t < 3; ++t) oa[t] = f32x4{0.f, 0.f, 0.f, 0.f};
    int la = L_RED + dpart * 768 + d4 * 16;
    asm volatile("" : "+v"(la));
    sfor<24>([&](auto J) INL {
      constexpr int j = decltype(J)::value;
      oa[j % 3] = __builtin_amdgcn_mfma_f32_16x16x32_bf16(__builtin_bit_cast(bf16x8, pop[j / 3]), __builtin_bit_cast(bf16x8, vd[j]), oa[j % 3], 0, 0, 0);
      PIN();
      if constexpr (24 + j + LAG >= N2 && 24 + j + LAG < 48 + CTOT) paced(IC(24 + j + LAG));
      if constexpr (j == 23 - EARLY) BARRIER();
    });
    if (dvalid) {
#pragma unroll
      for (int t = 0; t < 3; ++t) *(LAS f32x4*)(L + la + t * 256) = oa[t];
    }
    STAMP(9);
    BARRIER();
    {
      const v2u hh = *(LAS v2u*)(L + L_H + (16 * w + 2 * g8) * 4);
      const f32x4 ba = gemv(hh.y, mask, wc[1], gemv(hh.x, mask, wc[0], f32x4{0.f, 0.f, 0.f, 0.f}));
      if (dvalid) *(LAS f32x4*)(L + L_RB + dpart * 256 + d4 * 16) = ba;
    }
    BARRIER();
    ISSUE(48 + CTOT, 62, paced);
    STAMP(10);
    const rsrc_t rws = mk(p.ws, WS_END);
    int ooff = WS_O + (h * 8 + g8) * 1536 + (24 * w + c8) * 16, ls = L_RS + w * 320 + (2 * c16 + g16) * 16;
    int s_off = (WS_S + (20 * s + (lane >> 1)) * 1024 + h * 32 + (lane & 1) * 16) | (lane < 40 ? 0 : OOB), lr = L_RS + (lane < 40 ? lane : 0) * 8;
    asm volatile("" : "+v"(ooff), "+v"(ls), "+v"(s_off), "+v"(lr));
    v4u og[3];
    BARRIER();
    STAMP(21);
#pragma unroll
    for (int j = 0; j < 3; ++j) og[j] = LD4(rws, ooff + 128 * j, 0, SC1);
    BARRIER();
    const float f = *(LAS float*)(L + L_F + g8 * 4);
#define OBAD() ((STALE(og[0]) | STALE(og[1]) | STALE(og[2])) != 0)
    if (__builtin_expect(__any(OBAD()), 0)) {
      STAMP(18);
#pragma nounroll
      for (unsigned spins = 0; spins < SPIN_CAP; ++spins) {
        RELOAD();
        if (OBAD()) {
#pragma unroll
          for (int j = 0; j < 3; ++j) og[j] = LD4(rws, ooff + 128 * j, 0, SC1);
        }
        if (!__any(OBAD())) break;
      }
    }
    STAMP(11);
    {
      int x[3];
#pragma unroll
      for (int j = 0; j < 3; ++j) {
        float r = sum_x16(fold32(f * asf(og[j].y), f * asf(og[j].w)));
        r += dpp<ROR8>(r);
        x[j] = (int)pk(r, r);
        x[j] = __builtin_amdgcn_update_dpp(x[j], x[j], ROR4, 0xa, 0xf, false);
      }
      PIN();
      f32x4 sa = {0.f, 0.f, 0.f, 0.f};
      sfor<12>([&](auto N) INL {
        constexpr int n = decltype(N)::value;
        sa = gemv((unsigned)__builtin_amdgcn_mov_dpp(x[n / 4], BCAST + n % 4, 0xf, 0xf, true), mask, wc[2 + n], sa);
      });
      if (c16 < 10 && hi == 0) *(LAS f32x4*)(L + ls) = sa;
    }
    STAMP(12);
    BARRIER();
    if (w == 1) {
      const f32x2 a = (*(LAS f32x2*)(L + lr) + *(LAS f32x2*)(L + lr + 320)) + (*(LAS f32x2*)(L + lr + 640) + *(LAS f32x2*)(L + lr + 960));
      PUT(a.x, a.y, s_off);
      STAMP(13);
    }
  }
}
}

extern "C" void kernel_launch(void* const* d_in, const int* in_sizes, int n_in, void* d_out, int out_size, void* d_ws, size_t ws_size,
                              hipStream_t stream) {
  Params p;
  p.x = (const unsigned short*)d_in[0];
  p.k = (const unsigned char*)d_in[1];
  p.v = (const unsigned char*)d_in[2];
  p.wq = (const unsigned char*)d_in[3];
  p.wo = (const unsigned char*)d_in[4];
  p.wg = (const unsigned char*)d_in[6];
  p.wu = (const unsigned char*)d_in[7];
  p.wd = (const unsigned char*)d_in[8];
  p.out = (unsigned short*)d_out;
  p.ws = (unsigned char*)d_ws;
  hipLaunchKernelGGL(fused_decode, dim3(256), dim3(320), 0, stream, p.wq, p.wg, p.wu, p.x, p.ws, p.k, p.v, p.wo, p.wd, p.out);
}
```

```cpp
#include <hip/hip_runtime.h>
#include <hip/hip_bf16.h>
#include <utility>

namespace {
typedef unsigned v4u __attribute__((ext_vector_type(4)));
typedef unsigned v2u __attribute__((ext_vector_type(2)));
typedef unsigned v16u __attribute__((ext_vector_type(16)));
typedef float f32x4 __attribute__((ext_vector_type(4)));
typedef float f32x2 __attribute__((ext_vector_type(2)));
typedef __bf16 bf16x8 __attribute__((ext_vector_type(8)));
typedef __bf16 bf16x2 __attribute__((ext_vector_type(2)));
typedef unsigned long long u64;
typedef __amdgpu_buffer_rsrc_t rsrc_t;
#define LAS __attribute__((address_space(3)))

constexpr int D = 640, H = 32, DH = 192, SK = 8192, WIN = 2048, F = 1536;
constexpr int NT = 2, SC1 = 16;
constexpr int OOB = 0x40000000;
constexpr unsigned SPIN_CAP = 1u << 16;
constexpr float LOG2E = 1.4426950408889634f;
constexpr int CODE_BYTES = 10624;

#define TAU 143
#define TAUC 85
#define TOFF 1330
#define STAG 11
#define TFIN 1750
#define SW 13
#define NA0 3
#define DA 6
#define NQ 33
#define CTOT 9
#define EARLY 5
constexpr int due_ns(int n) { return TOFF + (n <= 50 ? TAU * n : TAU * 50 + TAUC * (n - 50)) + (n < 48 ? 0 : 40); }

constexpr int WS_EPOCH = 0;
constexpr int WS_Q = 4096;
constexpr int WS_G = WS_Q + 32 * 5120;
constexpr int WS_O = WS_G + 24 * 5120;
constexpr int WS_M = WS_O + 256 * 1536;
constexpr int WS_B = WS_M + 256 * 128;
constexpr int WS_S = WS_B + 24 * 5120;
constexpr int WS_HQ = WS_S + 160 * 1024;
constexpr int WS_HO = WS_HQ + 32 * 128;
constexpr int WS_END = WS_HO + 32 * 128;
constexpr int WS_STAMP = 2 << 20;

constexpr int L_RED = 0;
constexpr int L_X = 8192;
constexpr int L_Q = L_X + 2560;
constexpr int L_H = L_Q + 384;
constexpr int L_M = L_H + 256;
constexpr int L_TAG = L_M + 32;
constexpr int L_F = L_M + 64;
constexpr int L_RB = L_F + 32;
constexpr int L_RS = L_RB + 2048;
constexpr int L_DUMP = L_RS + 1280;
constexpr int LDS_BYTES = 88 * 1024;

struct Params {
  const unsigned char* wq; const unsigned char* wg; const unsigned char* wu; const unsigned char* k; const unsigned char* v;
  const unsigned char* wo; const unsigned char* wd; const unsigned short* x; unsigned short* out; unsigned char* ws;
};

__device__ __forceinline__ rsrc_t mk(const void* p, int bytes) { return __builtin_amdgcn_make_buffer_rsrc((void*)p, 0, bytes, 0x00020000); }
__device__ __forceinline__ unsigned pk(float lo, float hi) { bf16x2 v = {(__bf16)lo, (__bf16)hi}; return __builtin_bit_cast(unsigned, v); }
__device__ __forceinline__ float lo16(unsigned a) { return __builtin_bit_cast(float, a << 16); }
__device__ __forceinline__ float hi16(unsigned a) { return __builtin_bit_cast(float, a & 0xffff0000u); }
__device__ __forceinline__ float rbf(float a) { return lo16(pk(a, a)); }
__device__ __forceinline__ float asf(unsigned a) { return __builtin_bit_cast(float, a); }
__device__ __forceinline__ unsigned asu(float a) { return __builtin_bit_cast(unsigned, a); }
template <int CTRL> __device__ __forceinline__ float dpp(float x) {
  return __builtin_bit_cast(float, __builtin_amdgcn_mov_dpp(__builtin_bit_cast(int, x), CTRL, 0xf, 0xf, true));
}
constexpr int XOR1 = 0xB1, XOR2 = 0x4E, XOR7 = 0x141, ROR8 = 0x128, ROR4 = 0x124, BCAST = 0x150;
__device__ __forceinline__ float sum8(float x) { x += dpp<XOR1>(x); x += dpp<XOR2>(x); x += dpp<XOR7>(x); return x; }
__device__ __forceinline__ float max8(float x) { x = fmaxf(x, dpp<XOR1>(x)); x = fmaxf(x, dpp<XOR2>(x)); x = fmaxf(x, dpp<XOR7>(x)); return x; }
__device__ __forceinline__ float sum_x16(float x) { auto s = __builtin_amdgcn_permlane16_swap(asu(x), asu(x), false, false); return asf(s[0]) + asf(s[1]); }
__device__ __forceinline__ float sum_x32(float x) { auto s = __builtin_amdgcn_permlane32_swap(asu(x), asu(x), false, false); return asf(s[0]) + asf(s[1]); }
__device__ __forceinline__ float fold32(float a, float b) { auto s = __builtin_amdgcn_permlane32_swap(asu(a), asu(b), false, false); return asf(s[0]) + asf(s[1]); }
__device__ __forceinline__ float max_x16(float x) { auto s = __builtin_amdgcn_permlane16_swap(asu(x), asu(x), false, false); return fmaxf(asf(s[0]), asf(s[1])); }
__device__ __forceinline__ float max_x32(float x) { auto s = __builtin_amdgcn_permlane32_swap(asu(x), asu(x), false, false); return fmaxf(asf(s[0]), asf(s[1])); }
__device__ __forceinline__ float sum_groups(float x) { x += dpp<ROR8>(x); return sum_x32(sum_x16(x)); }
__device__ __forceinline__ float dot2(unsigned a, unsigned b, float c) {
  return __builtin_amdgcn_fdot2_f32_bf16(__builtin_bit_cast(bf16x2, a), __builtin_bit_cast(bf16x2, b), c, false);
}
__device__ __forceinline__ f32x4 gemv(unsigned pp, v4u mask, v4u b, f32x4 c) {
  const v4u a = {pp & mask.x, pp & mask.y, pp & mask.z, pp & mask.w};
  return __builtin_amdgcn_mfma_f32_16x16x32_bf16(__builtin_bit_cast(bf16x8, a), __builtin_bit_cast(bf16x8, b), c, 0, 0, 0);
}
template <class Fn, int... I> __device__ __forceinline__ void sfor_(Fn&& f, std::integer_sequence<int, I...>) { (f(std::integral_constant<int, I>{}), ...); }
template <int N, class Fn> __device__ __forceinline__ void sfor(Fn&& f) { sfor_(f, std::make_integer_sequence<int, N>{}); }
#define INL __attribute__((always_inline))
#define IC(n) std::integral_constant<int, (n)>{}
#define RELOAD() asm volatile("" ::: "memory")
#define LD4(rs, vo, so, aux) __builtin_amdgcn_raw_buffer_load_b128(rs, vo, so, aux)
#define PUT(a, b, vo) __builtin_amdgcn_raw_buffer_store_b128(v4u{tlo, asu(a), thi, asu(b)}, rws, vo, 0, SC1)
#define HINT(off) __builtin_amdgcn_raw_buffer_store_b32(tlo, rws, lane == 0 ? (off) : OOB, 0, SC1)
#define STALE(g) (((g).x ^ tlo) | ((g).z ^ thi))
#define CONFIRM(g, off, live) do { if (__builtin_expect(__any((live) && STALE(g) != 0), 0)) { _Pragma("nounroll") \
    for (unsigned spins = 0; spins < SPIN_CAP; ++spins) { \
      asm volatile("buffer_load_dwordx4 %0, %1, %2, 0 offen sc1\n\ts_waitcnt vmcnt(0)" : "+v"(g) : "v"(off), "s"(rws) : "memory"); \
      if (!__any((live) && STALE(g) != 0)) break; } } } while (0)
#define PIN() __builtin_amdgcn_sched_barrier(0)
#define BARRIER() do { PIN(); asm volatile("s_waitcnt lgkmcnt(0)\n\ts_barrier" ::: "memory"); PIN(); } while (0)
#define STAMP(i) do { } while (0)

template <int N> __device__ __forceinline__ void wait_hint(const unsigned char* ip, unsigned t) {
  typedef u64 v8l __attribute__((ext_vector_type(8)));
  const u64 tt = ((u64)t << 32) | t;
#pragma nounroll
  for (int spins = 0; spins < 256; ++spins) {
    v8l v;
    asm volatile("s_load_dwordx16 %0, %1, 0x0 glc\n\ts_waitcnt lgkmcnt(0)" : "=&s"(v) : "s"(ip) : "memory");
    u64 bad = 0;
#pragma unroll
    for (int i = 0; i < N / 2; ++i) bad |= v[i] ^ tt;
    asm volatile("" : "+s"(bad), "+s"(spins));
    if (bad == 0) break;
  }
}

__global__ void __launch_bounds__(320, 2) __attribute__((aligned(4096))) fused_decode(const unsigned char* a_wq, const unsigned char* a_wg, const unsigned char* a_wu, const unsigned short* a_x,
    unsigned char* a_ws, const unsigned char* a_k, const unsigned char* a_v, const unsigned char* a_wo, const unsigned char* a_wd, unsigned short* a_out) {
  __shared__ __attribute__((aligned(16))) unsigned char smem[LDS_BYTES];
  LAS unsigned char* L = (LAS unsigned char*)smem;
  STAMP(0);
  v16u ka; v4u kb;
  asm volatile("s_load_dwordx16 %0, %2, 0x0\n\ts_load_dwordx4 %1, %2, 0x40" : "=&s"(ka), "=&s"(kb) : "s"(__builtin_amdgcn_kernarg_segment_ptr()));
  const int b = blockIdx.x, tid = threadIdx.x, lane = tid & 63, w = __builtin_amdgcn_readfirstlane(tid >> 6);
  unsigned pf0, pf1;
  {
    const rsrc_t rc = mk((const void*)(__builtin_amdgcn_s_getpc() & ~127ull), CODE_BYTES);
    const int po = (tid ^ 256) * 128;
    pf0 = __builtin_amdgcn_raw_buffer_load_b32(rc, po, 0, 0);
    pf1 = __builtin_amdgcn_raw_buffer_load_b32(rc, po + 8192, 0, 0);
  }
  if (w == 4) __builtin_amdgcn_s_setprio(3);
  PIN();
  const int idx = b >> 3, h = 4 * (b & 7) + (idx & 3), s = idx >> 2;
  const int f3 = (idx * 26) >> 8, cf = 3 * (b & 7) + f3, fj = idx - 10 * f3, fo = idx < 30 ? 0 : OOB;
  const int g8 = lane >> 3, c8 = lane & 7, g16 = lane >> 4, c16 = lane & 15, hi = lane >> 5, l32 = lane & 31;
  const int dpart = 2 * w + (g8 & 1);
  const int R = 240 * s - (s & 4) * 4 + 32 * dpart + 8 * g16, cb = (R >= 640) + (R >= 1280), rq0 = R - 640 * cb;
  const int rf0 = 128 * (fj - 5 * (fj >= 5)) + 32 * w + 4 * g8;
  const bool rok = (unsigned)(R - 240 * s) < 240u;
  int vq = (rq0 * 12288 + h * 384 + cb * 128 + c8 * 16) | (rok ? 0 : OOB), vf = (rf0 * 3072 + cf * 128 + c8 * 16) | fo;
  int vkv = (h * SK + (SK - WIN) + 32 * s + 8 * w + g8) * 384 + c8 * 16;
  int vx = lane * 20;
  v4u xa;
  v2u ep;
  unsigned xb, t_own;
  int role = w;
  asm volatile("" : "+v"(vq), "+v"(vf), "+v"(vkv), "+v"(vx), "=v"(xa), "=v"(xb), "=v"(ep));
  asm volatile("" : "=s"(t_own), "+s"(role));
  PIN();
  asm volatile("s_waitcnt lgkmcnt(0)" : "+s"(ka), "+s"(kb) :: "memory");
#define ARG(v, i) (unsigned char*)(((u64)(v)[2 * (i) + 1] << 32) | (v)[2 * (i)])
  const Params p = {ARG(ka, 0), ARG(ka, 1), ARG(ka, 2), ARG(ka, 5), ARG(ka, 6), ARG(ka, 7), ARG(kb, 0), (const unsigned short*)ARG(ka, 3), (unsigned short*)ARG(kb, 1), ARG(ka, 4)};
  if (w == 4) {
    const rsrc_t rx = mk(p.x, D * 2);
    xa = LD4(rx, vx, 0, 0);
    xb = __builtin_amdgcn_raw_buffer_load_b32(rx, vx + 16, 0, 0);
    ep = __builtin_amdgcn_raw_buffer_load_b64(mk(p.ws, WS_END), WS_EPOCH, 0, SC1);
    u64 t;
    asm volatile("s_memrealtime %0\n\ts_waitcnt lgkmcnt(0)" : "=s"(t) :: "memory");
    t_own = (unsigned)t;
  }

  if (role == 4) {
    const rsrc_t rws = mk(p.ws, WS_END), rx = mk(p.x, D * 2);
    const int rj = 16 * ((b & 7) + (idx >> 4) * 8) + (idx & 15);
    const unsigned short xo = __builtin_amdgcn_raw_buffer_load_b16(rx, (4 * rj + 2 * (lane & 1) + hi) * 2, 0, 0);
    {
      float xv[10] = {lo16(xa.x), hi16(xa.x), lo16(xa.y), hi16(xa.y), lo16(xa.z), hi16(xa.z), lo16(xa.w), hi16(xa.w), lo16(xb), hi16(xb)};
      float sm = 0.f;
#pragma unroll
      for (int i = 0; i < 10; ++i) sm += xv[i];
      const float mean = sum_groups(sum8(sm)) * (1.f / D);
      float sv = 0.f;
#pragma unroll
      for (int i = 0; i < 10; ++i) { const float d = xv[i] - mean; sv = fmaf(d, d, sv); }
      const float var = sum_groups(sum8(sv)) * (1.f / D);
      const float mb = rbf(mean), sd = rbf(__builtin_amdgcn_sqrtf(rbf(rbf(var) + 1e-5f))), rs = __builtin_amdgcn_rcpf(sd);
      unsigned xp[10];
#pragma unroll
      for (int i = 0; i < 10; ++i) { const float d = rbf(xv[i] - mb), q = d * rs, r = fmaf(fmaf(-q, sd, d), rs, q); xp[i] = pk(r, r); }
#pragma unroll
      for (int i = 0; i < 5; ++i) *(LAS v2u*)(L + L_X + lane * 40 + i * 8) = v2u{xp[2 * i], xp[2 * i + 1]};
    }
    STAMP(1);
    BARRIER();
    const u64 tag = (((u64)__builtin_amdgcn_readfirstlane(ep.y) << 32) | (unsigned)__builtin_amdgcn_readfirstlane(ep.x)) + 0x9E3779B97F4A7C15ull;
    const unsigned tlo = (unsigned)tag, thi = (unsigned)(tag >> 32);
    *(LAS v2u*)(L + L_TAG) = v2u{tlo, thi};
    if (lane < 8) *(LAS float*)(L + L_M + lane * 4) = -1.f;
    asm volatile("" :: "v"(pf0), "v"(pf1));
    const int nd = s == 2 ? 5 : s == 5 ? 3 : 8;
    int q_off = (WS_Q + h * 5120 + (s + (3 * s >> 3)) * 512 + lane * 16) | (hi == 0 || nd < 8 ? 0 : OOB);
    int g_off = (WS_G + cf * 5120 + fj * 512 + lane * 16) | (hi == 0 ? fo : OOB);
    int hq_off = lane == 0 ? WS_HQ + h * 128 + s * 4 : OOB;
    asm volatile("" : "+v"(q_off), "+v"(g_off), "+v"(hq_off));
    STAMP(7);
    BARRIER();
    STAMP(2);
    {
      f32x2 t[8], c[8];
#pragma unroll
      for (int i = 0; i < 8; ++i) { t[i] = *(LAS f32x2*)(L + L_RED + i * 512 + l32 * 8); c[i] = *(LAS f32x2*)(L + L_RED + i * 512 + 256 + l32 * 8); }
      f32x2 s1 = t[0] + t[1] + t[2], s2 = t[3] + t[4], s3 = t[5] + t[6] + t[7];
      asm volatile("" : "+v"(s1), "+v"(s2), "+v"(s3));
      const f32x2 z = {0.f, 0.f};
      const f32x2 a = (hi ? z : s1) + ((nd == 3) == (hi != 0) ? s2 : z) + ((nd == 8) == (hi == 0) ? s3 : z);
      PUT(a.x, a.y, q_off);
      __builtin_amdgcn_raw_buffer_store_b32(tlo, rws, hq_off, 0, SC1);
      const f32x2 g = ((c[0] + c[1]) + (c[2] + c[3])) + ((c[4] + c[5]) + (c[6] + c[7]));
      PUT(g.x, g.y, g_off);
    }
    STAMP(3);
    {
      v4u r[5];
      wait_hint<8>(p.ws + WS_HQ + h * 128, tlo);
      STAMP(17);
#pragma nounroll
      for (unsigned spins = 0; spins < SPIN_CAP; ++spins) {
        RELOAD();
#pragma unroll
        for (int i = 0; i < 5; ++i) r[i] = LD4(rws, WS_Q + h * 5120 + lane * 16, i * 1024, SC1);
        if (!__any((STALE(r[0]) | STALE(r[1]) | STALE(r[2]) | STALE(r[3]) | STALE(r[4])) != 0)) break;
      }
      STAMP(4);
      const float a0 = asf(r[0].y) + (hi ? 0.f : asf(r[1].y)), a1 = asf(r[0].w) + (hi ? 0.f : asf(r[1].w));
      const float b0 = asf(r[2].y) + asf(hi ? r[1].y : r[3].y), b1 = asf(r[2].w) + asf(hi ? r[1].w : r[3].w);
      const float c0 = asf(r[4].y) + (hi ? asf(r[3].y) : 0.f), c1 = asf(r[4].w) + (hi ? asf(r[3].w) : 0.f);
      *(LAS unsigned*)(L + L_Q + lane * 4) = pk(fold32(a0, b0), fold32(a1, b1));
      *(LAS unsigned*)(L + L_Q + 256 + l32 * 4) = pk(sum_x32(c0), sum_x32(c1));
    }
    STAMP(5);
    BARRIER();
    if (idx < 30) {
      v4u r[5];
#pragma nounroll
      for (unsigned spins = 0; spins < SPIN_CAP; ++spins) {
        RELOAD();
#pragma unroll
        for (int i = 0; i < 5; ++i) r[i] = LD4(rws, WS_G + cf * 5120 + lane * 16, i * 1024, SC1);
        if (!__any((STALE(r[0]) | STALE(r[1]) | STALE(r[2]) | STALE(r[3]) | STALE(r[4])) != 0)) break;
      }
      const float g0 = sum_x32(asf(r[0].y) + asf(r[1].y) + (hi ? 0.f : asf(r[2].y))), g1 = sum_x32(asf(r[0].w) + asf(r[1].w) + (hi ? 0.f : asf(r[2].w)));
      const float u0 = sum_x32(asf(r[3].y) + asf(r[4].y) + (hi ? asf(r[2].y) : 0.f)), u1 = sum_x32(asf(r[3].w) + asf(r[4].w) + (hi ? asf(r[2].w) : 0.f));
      const float h0 = rbf(g0) * rbf(u0), h1 = rbf(g1) * rbf(u1);
      *(LAS v2u*)(L + L_H + l32 * 8) = v2u{pk(h0, h0), pk(h1, h1)};
    } else {
      *(LAS v2u*)(L + L_H + l32 * 8) = v2u{0u, 0u};
    }
    STAMP(6);
    float wt[4];
    {
      f32x4 a, c;
#pragma nounroll
      for (unsigned spins = 0; spins < (SPIN_CAP << 4); ++spins) {
        RELOAD();
        a = *(LAS f32x4*)(L + L_M); c = *(LAS f32x4*)(L + L_M + 16);
        if (!__any(a.y < 0.f || a.w < 0.f || c.y < 0.f || c.w < 0.f)) break;
        __builtin_amdgcn_s_sleep(2);
      }
      const float mx = fmaxf(fmaxf(a.x, a.z), fmaxf(c.x, c.z));
      wt[0] = __builtin_amdgcn_exp2f((a.x - mx) * LOG2E); wt[1] = __builtin_amdgcn_exp2f((a.z - mx) * LOG2E);
      wt[2] = __builtin_amdgcn_exp2f((c.x - mx) * LOG2E); wt[3] = __builtin_amdgcn_exp2f((c.z - mx) * LOG2E);
      PUT(mx, (wt[0] * a.y + wt[1] * a.w) + (wt[2] * c.y + wt[3] * c.w), lane == 0 ? WS_M + (h * 8 + s) * 128 : OOB);
    }
    int o_off = WS_O + (h * 8 + s) * 1536 + lane * 16, o_of1 = (o_off + 1024) | (hi ? OOB : 0);
    asm volatile("" : "+v"(o_off), "+v"(o_of1), "+v"(wt[0]), "+v"(wt[1]), "+v"(wt[2]), "+v"(wt[3]));
    STAMP(8);
    BARRIER();
    HINT(WS_HO + h * 128 + s * 4);
    const int moff = (WS_M + (h * 8 + c8) * 128) | (lane < 8 ? 0 : OOB);
    v4u ml = LD4(rws, moff, 0, SC1);
    STAMP(9);
    BARRIER();
    {
      f32x2 t[8], u[8];
#pragma unroll
      for (int i = 0; i < 8; ++i) t[i] = *(LAS f32x2*)(L + L_RED + i * 1024 + lane * 8);
#pragma unroll
      for (int i = 0; i < 8; ++i) u[i] = *(LAS f32x2*)(L + L_RED + i * 1024 + 512 + l32 * 8);
      PIN();
      const f32x2 a = (wt[0] * (t[0] + t[1]) + wt[1] * (t[2] + t[3])) + (wt[2] * (t[4] + t[5]) + wt[3] * (t[6] + t[7]));
      const f32x2 c = (wt[0] * (u[0] + u[1]) + wt[1] * (u[2] + u[3])) + (wt[2] * (u[4] + u[5]) + wt[3] * (u[6] + u[7]));
      PUT(a.x, a.y, o_off);
      PUT(c.x, c.y, o_of1);
    }
    STAMP(10);
    BARRIER();
    STAMP(16);
    wait_hint<8>(p.ws + WS_HO + h * 128, tlo);
    STAMP(22);
    asm volatile("s_waitcnt vmcnt(0)" ::: "memory");
    STAMP(12);
    BARRIER();
    {
      CONFIRM(ml, moff, lane < 8);
      const float M = max8(asf(ml.y));
      const float e = __builtin_amdgcn_exp2f((asf(ml.y) - M) * LOG2E);
      const float f = e * __builtin_amdgcn_rcpf(sum8(e * asf(ml.w)));
      if (lane < 8) *(LAS float*)(L + L_F + lane * 4) = f;
    }
    STAMP(11);
    BARRIER();
    {
      f32x2 a[8];
#pragma unroll
      for (int i = 0; i < 8; ++i) a[i] = *(LAS f32x2*)(L + L_RB + i * 256 + l32 * 8);
      const f32x2 t = ((a[0] + a[1]) + (a[2] + a[3])) + ((a[4] + a[5]) + (a[6] + a[7]));
      PUT(t.x, t.y, (WS_B + cf * 5120 + fj * 512 + lane * 16) | (hi == 0 ? fo : OOB));
    }
    const int boff = (WS_B + (lane >> 1) * 5120 + rj * 32 + (lane & 1) * 16) | (lane < 48 ? 0 : OOB);
    BARRIER();
    if (rj < 160) {
      v4u r = LD4(rws, boff, 0, SC1);
      CONFIRM(r, boff, lane < 48);
      float t = fold32(asf(r.y), asf(r.w));
      t += dpp<XOR2>(t); t += dpp<ROR4>(t); t += dpp<ROR8>(t); t = sum_x16(t);
      const float bv = rbf(t);
      float gate = rbf(bv / (1.f + __expf(-bv))), xv = lo16(xo);
      int oo = l32 < 2 ? (4 * rj + 2 * (lane & 1) + hi) * 2 : OOB;
      int r_off = WS_S + rj * 1024 + lane * 16;
      rsrc_t rout = mk(p.out, D * 2);
      asm volatile("" : "+v"(gate), "+v"(xv), "+v"(oo), "+v"(r_off));
      asm volatile("" : "+s"(rout));
      STAMP(19);
      asm volatile("1:\n\ts_memrealtime vcc\n\ts_waitcnt lgkmcnt(0)\n\ts_sub_u32 vcc_lo, vcc_lo, %0\n\t"
                   "s_cmpk_lt_u32 vcc_lo, %1\n\ts_cbranch_scc1 1b" :: "s"(t_own), "n"((due_ns(61) + TFIN) / 10) : "vcc", "scc", "memory");
      {
        unsigned left = SPIN_CAP;
        u64 tmp;
        asm volatile("1:\n\tbuffer_load_dwordx4 v[40:43], %3, %4, 0 offen sc1\n\ts_sub_u32 %1, %1, 1\n\ts_waitcnt vmcnt(0)\n\ts_cbranch_scc1 2f\n\t"
                     "v_cmp_ne_u32 vcc, %5, v40\n\tv_cmp_ne_u32 %2, %6, v42\n\ts_or_b64 vcc, vcc, %2\n\ts_cbranch_scc1 1b\n2:"
                     : "=&{v[40:43]}"(r), "+s"(left), "=&s"(tmp) : "v"(r_off), "s"(rws), "s"(tlo), "s"(thi) : "vcc", "scc", "memory");
      }
      STAMP(14);
      t = fold32(asf(r.y), asf(r.w));
      t += dpp<XOR2>(t); t += dpp<ROR4>(t); t += dpp<ROR8>(t); t = sum_x16(t);
      const float res = xv + rbf(rbf(t) * gate);
      __builtin_amdgcn_raw_buffer_store_b16((unsigned short)pk(res, res), rout, oo, 0, 0);
      if (b == 0) __builtin_amdgcn_raw_buffer_store_b64(v2u{tlo, thi}, rws, lane == 0 ? WS_EPOCH : OOB, 0, SC1);
    }
  } else {
    STAMP(17);
    v4u wa[12];
    u64 t_entry;
    asm volatile("s_memrealtime %0" : "=s"(t_entry) :: "memory");
    PIN();
    {
      const rsrc_t rq = mk(p.wq, D * H * DH * 2);
#pragma unroll
      for (int i = 0; i < 8; ++i) { wa[i] = LD4(rq, vq, i * 12288, NT); PIN(); }
      const rsrc_t rf = mk(fj < 5 ? p.wg : p.wu, D * F * 2);
#pragma unroll
      for (int i = 0; i < 4; ++i) { wa[8 + i] = LD4(rf, vf, i * 3072, NT); PIN(); }
    }
    const rsrc_t rk = mk(p.k, H * SK * DH * 2), rv = mk(p.v, H * SK * DH * 2), ro = mk(p.wo, H * DH * D * 2), rd = mk(p.wd, F * D * 2);
    const int vo = ((192 * h + 48 * w + 8 * (g16 & 1) + (g16 >> 1)) * 1280 + (10 * s + c16) * 16) | (c16 < 10 ? 0 : OOB);
    const int vdn = ((64 * cf + 16 * w + 2 * g8) * 1280 + fj * 128 + c8 * 16) | fo;
    asm volatile("s_waitcnt lgkmcnt(0)" : "+s"(t_entry) :: "memory");
    const unsigned t_start = (unsigned)t_entry, t_stag = t_start + (h & 3) * STAG;
    v4u kd[24], vd[24], wc[14];
    auto issue = [&](auto N) INL {
      constexpr int n = decltype(N)::value;
      if constexpr (n < 24) kd[n] = LD4(rk, vkv + (n % 3) * 128, ((n / 3 + h) & 7) * 98304, NT);
      else if constexpr (n < 48) vd[n - 24] = LD4(rv, vkv + (n % 3) * 128, ((n / 3 + h) & 7) * 98304, NT);
      else if constexpr (n < 50) wc[n - 48] = LD4(rd, vdn, (n - 48) * 1280, NT);
      else wc[n - 48] = LD4(ro, vo, (16 * ((n - 50) >> 2) + 2 * ((n - 50) & 3)) * 1280, NT);
      PIN();
    };
    auto paced = [&](auto N) INL {
      constexpr int n = decltype(N)::value;
      constexpr unsigned due = due_ns(n) / 10;
      if constexpr (n >= 48 || n % 3 == 0)
        asm volatile("s_waitcnt vmcnt(%1)\n1:\n\ts_memrealtime vcc\n\ts_waitcnt lgkmcnt(0)\n\ts_sub_u32 vcc_lo, vcc_lo, %0\n\t"
                   "s_cmpk_lt_u32 vcc_lo, %2\n\ts_cbranch_scc1 1b" :: "s"(n < 48 ? t_stag : t_start), "n"(SW - (n < 48 ? 3 : 1)), "n"(due) : "vcc", "scc", "memory");
      PIN();
      issue(N);
    };
#define ISSUE(lo, hi, fn) sfor<(hi) - (lo)>([&](auto I_) INL { fn(IC((lo) + decltype(I_)::value)); })
    v4u mask;
    {
      const unsigned hm = (lane & 1) ? 0xffff0000u : 0x0000ffffu;
      const int dsel = c8 >> 1;
      mask = v4u{dsel == 0 ? hm : 0u, dsel == 1 ? hm : 0u, dsel == 2 ? hm : 0u, dsel == 3 ? hm : 0u};
    }
    asm volatile("" :: "v"(pf0), "v"(pf1));
    ISSUE(0, NA0, paced);
    STAMP(1);
    BARRIER();
    v4u xn[3];
    xn[0] = *(LAS v4u*)(L + L_X + (rok ? rq0 : 0) * 4); xn[1] = *(LAS v4u*)(L + L_X + (rok ? rq0 : 0) * 4 + 16); xn[2] = *(LAS v4u*)(L + L_X + rf0 * 4);
    f32x4 qa = {0.f, 0.f, 0.f, 0.f}, fa = {0.f, 0.f, 0.f, 0.f};
    sfor<12>([&](auto J) INL {
      constexpr int j = decltype(J)::value;
      if constexpr (j < 8) qa = gemv(xn[j / 4][j % 4], mask, wa[j], qa); else fa = gemv(xn[2][j % 4], mask, wa[j], fa);
      PIN();
      if constexpr (j >= 11 - DA + NA0 && j < 11) paced(IC(j - (11 - DA)));
    });
    const bool dvalid = hi == (g8 & 1);
    const int d4 = 2 * c8 + (g16 & 1);
    if (dvalid) { *(LAS f32x4*)(L + L_RED + dpart * 512 + d4 * 16) = qa; *(LAS f32x4*)(L + L_RED + dpart * 512 + 256 + d4 * 16) = fa; }
    STAMP(2);
    BARRIER();
    ISSUE(DA, NQ, paced);
    STAMP(5);
    BARRIER();
    STAMP(6);
    constexpr int N1 = NQ, N2 = NQ + 7;
    v4u qf[3];
#pragma unroll
    for (int t = 0; t < 3; ++t) qf[t] = *(LAS v4u*)(L + L_Q + t * 128 + c8 * 16);
    const v2u tg = *(LAS v2u*)(L + L_TAG);
    const unsigned tlo = tg.x, thi = tg.y;
    float sc[8];
#pragma unroll
    for (int i = 0; i < 8; ++i) sc[i] = 0.f;
    sfor<3>([&](auto T) INL {
      constexpr int t = decltype(T)::value;
#pragma unroll
      for (int e = 0; e < 4; ++e) {
#pragma unroll
        for (int i = 0; i < 8; ++i) sc[i] = dot2(kd[3 * i + t][e], qf[t][e], sc[i]);
        PIN();
      }
      if constexpr (t > 0) paced(IC(N1 + t - 1));
    });
    sfor<2>([&](auto T) INL {
      constexpr int t = decltype(T)::value;
#pragma unroll
      for (int i = 4 * t; i < 4 * t + 4; ++i) { sc[i] = rbf(rbf(sum8(sc[i])) * 0.07216878364870322f); asm volatile("" : "+v"(sc[i])); }
      PIN();
      paced(IC(N1 + 2 + t));
    });
    float mx = -3.0e38f;
#pragma unroll
    for (int i = 0; i < 8; ++i) mx = fmaxf(mx, sc[i]);
    mx = fmaxf(mx, dpp<ROR8>(mx)); mx = max_x32(max_x16(mx));
    asm volatile("" : "+v"(mx));
    PIN();
    paced(IC(N1 + 4));
    v4u pop[8];
    float lsum = 0.f;
    sfor<2>([&](auto T) INL {
      constexpr int t = decltype(T)::value;
#pragma unroll
      for (int i = 4 * t; i < 4 * t + 4; ++i) {
        const float e = __builtin_amdgcn_exp2f((sc[i] - mx) * LOG2E);
        lsum += e;
        const unsigned pp = pk(e, e);
        pop[i] = v4u{pp & mask.x, pp & mask.y, pp & mask.z, pp & mask.w};
        asm volatile("" : "+v"(pop[i]));
      }
      PIN();
      paced(IC(N1 + 5 + t));
    });
    lsum = sum_groups(lsum);
    if (lane == 0) *(LAS f32x2*)(L + L_M + w * 8) = f32x2{mx, lsum};
    PIN();
    STAMP(8);
    f32x4 oa[3];
#pragma unroll
    for (int t = 0; t < 3; ++t) oa[t] = f32x4{0.f, 0.f, 0.f, 0.f};
    int la = dvalid ? L_RED + dpart * 1024 + d4 * 16 : L_DUMP + lane * 16;
    asm volatile("" : "+v"(la));
    sfor<24>([&](auto J) INL {
      constexpr int j = decltype(J)::value;
      oa[j % 3] = __builtin_amdgcn_mfma_f32_16x16x32_bf16(__builtin_bit_cast(bf16x8, pop[j / 3]), __builtin_bit_cast(bf16x8, vd[j]), oa[j % 3], 0, 0, 0);
      PIN();
      if constexpr (24 + j + SW >= N2 && 24 + j + SW < 48 + CTOT) paced(IC(24 + j + SW));
      if constexpr (j == 23 - EARLY) BARRIER();
    });
#pragma unroll
    for (int t = 0; t < 3; ++t) *(LAS f32x4*)(L + la + t * 256) = oa[t];
    STAMP(9);
    BARRIER();
    {
      const v2u hh = *(LAS v2u*)(L + L_H + (16 * w + 2 * g8) * 4);
      const f32x4 ba = gemv(hh.y, mask, wc[1], gemv(hh.x, mask, wc[0], f32x4{0.f, 0.f, 0.f, 0.f}));
      if (dvalid) *(LAS f32x4*)(L + L_RB + dpart * 256 + d4 * 16) = ba;
    }
    BARRIER();
    ISSUE(48 + CTOT, 62, issue);
    STAMP(10);
    const rsrc_t rws = mk(p.ws, WS_END);
    int ooff = WS_O + (h * 8 + g8) * 1536 + (24 * w + c8) * 16, ls = c16 < 10 && hi == 0 ? L_RS + w * 320 + (2 * c16 + g16) * 16 : L_DUMP + lane * 16;
    int s_off = (WS_S + (20 * s + (lane >> 1)) * 1024 + h * 32 + (lane & 1) * 16) | (lane < 40 ? 0 : OOB), lr = L_RS + (lane < 40 ? lane : 0) * 8;
    asm volatile("" : "+v"(ooff), "+v"(ls), "+v"(s_off), "+v"(lr));
    v4u og[3];
    BARRIER();
    STAMP(21);
#pragma unroll
    for (int j = 0; j < 3; ++j) og[j] = LD4(rws, ooff + 128 * j, 0, SC1);
    BARRIER();
    float f = *(LAS float*)(L + L_F + g8 * 4);
    asm volatile("" : "+v"(f));
    PIN();
    unsigned bad = STALE(og[0]) | STALE(og[1]) | STALE(og[2]);
    asm volatile("" : "+v"(bad));
    if (__builtin_expect(__any(bad != 0), 0)) {
      STAMP(18);
#pragma nounroll
      for (unsigned spins = 0; spins < SPIN_CAP; ++spins) {
        asm volatile("buffer_load_dwordx4 %0, %3, %4, 0 offen sc1\n\tbuffer_load_dwordx4 %1, %3, %4, 0 offen offset:128 sc1\n\t"
                     "buffer_load_dwordx4 %2, %3, %4, 0 offen offset:256 sc1\n\ts_waitcnt vmcnt(0)"
                     : "+v"(og[0]), "+v"(og[1]), "+v"(og[2]) : "v"(ooff), "s"(rws) : "memory");
        if (!__any((STALE(og[0]) | STALE(og[1]) | STALE(og[2])) != 0)) break;
      }
    }
    STAMP(11);
    {
      float y[3], z[3];
      int x[3];
#pragma unroll
      for (int j = 0; j < 3; ++j) { y[j] = f * asf(og[j].y); z[j] = f * asf(og[j].w); }
      PIN();
#pragma unroll
      for (int j = 0; j < 3; ++j) y[j] = fold32(y[j], z[j]);
      PIN();
#pragma unroll
      for (int j = 0; j < 3; ++j) y[j] += dpp<ROR8>(y[j]);
      PIN();
#pragma unroll
      for (int j = 0; j < 3; ++j) z[j] = dpp<ROR4>(y[j]);
      PIN();
#pragma unroll
      for (int j = 0; j < 3; ++j) { const auto t = __builtin_amdgcn_permlane16_swap(asu(y[j]), asu(z[j]), false, false); y[j] = asf(t[0]); z[j] = asf(t[1]); }
      PIN();
#pragma unroll
      for (int j = 0; j < 3; ++j) y[j] += z[j];
      PIN();
#pragma unroll
      for (int j = 0; j < 3; ++j) x[j] = (int)pk(y[j], y[j]);
      PIN();
      f32x4 sa = {0.f, 0.f, 0.f, 0.f};
      v4u op[13];
      auto build = [&](auto N) INL {
        constexpr int n = decltype(N)::value;
        const unsigned pp = (unsigned)__builtin_amdgcn_mov_dpp(x[n / 4], BCAST + n % 4, 0xf, 0xf, true);
        op[n] = v4u{pp & mask.x, pp & mask.y, pp & mask.z, pp & mask.w};
        PIN();
      };
      build(IC(0));
      sfor<12>([&](auto N) INL {
        constexpr int n = decltype(N)::value;
        if constexpr (n < 11) build(IC(n + 1));
        sa = __builtin_amdgcn_mfma_f32_16x16x32_bf16(__builtin_bit_cast(bf16x8, op[n]), __builtin_bit_cast(bf16x8, wc[2 + n]), sa, 0, 0, 0);
        PIN();
      });
      *(LAS f32x4*)(L + ls) = sa;
#pragma unroll
      for (int n = 2; n < 14; ++n) asm volatile("" :: "v"(wc[n]));
    }
    STAMP(12);
    BARRIER();
    if (w == 1) {
      const f32x2 a = (*(LAS f32x2*)(L + lr) + *(LAS f32x2*)(L + lr + 320)) + (*(LAS f32x2*)(L + lr + 640) + *(LAS f32x2*)(L + lr + 960));
      PUT(a.x, a.y, s_off);
      STAMP(13);
    }
  }
}
}

extern "C" void kernel_launch(void* const* d_in, const int* in_sizes, int n_in, void* d_out, int out_size, void* d_ws, size_t ws_size,
                              hipStream_t stream) {
  Params p;
  p.x = (const unsigned short*)d_in[0];
  p.k = (const unsigned char*)d_in[1];
  p.v = (const unsigned char*)d_in[2];
  p.wq = (const unsigned char*)d_in[3];
  p.wo = (const unsigned char*)d_in[4];
  p.wg = (const unsigned char*)d_in[6];
  p.wu = (const unsigned char*)d_in[7];
  p.wd = (const unsigned char*)d_in[8];
  p.out = (unsigned short*)d_out;
  p.ws = (unsigned char*)d_ws;
  hipLaunchKernelGGL(fused_decode, dim3(256), dim3(320), 0, stream, p.wq, p.wg, p.wu, p.x, p.ws, p.k, p.v, p.wo, p.wd, p.out);
}
```

```cpp
#include <hip/hip_runtime.h>
#include <hip/hip_bf16.h>
#include <utility>

namespace {
typedef unsigned v4u __attribute__((ext_vector_type(4)));
typedef unsigned v2u __attribute__((ext_vector_type(2)));
typedef unsigned v16u __attribute__((ext_vector_type(16)));
typedef float f32x4 __attribute__((ext_vector_type(4)));
typedef float f32x2 __attribute__((ext_vector_type(2)));
typedef __bf16 bf16x8 __attribute__((ext_vector_type(8)));
typedef __bf16 bf16x2 __attribute__((ext_vector_type(2)));
typedef unsigned long long u64;
typedef __amdgpu_buffer_rsrc_t rsrc_t;
#define LAS __attribute__((address_space(3)))

constexpr int D = 640, H = 32, DH = 192, SK = 8192, WIN = 2048, F = 1536;
constexpr int NT = 2, SC1 = 16;
constexpr int OOB = 0x40000000;
constexpr unsigned SPIN_CAP = 1u << 16;
constexpr float LOG2E = 1.4426950408889634f;
constexpr int CODE_BYTES = 10752;

#define TAU 144
#define TAUC 85
#define TOFF 1200
#define NAB 6
#define TA0 400
#define TA 125
#define STAG 11
#define TFIN 1750
#define SW 13
#define LAG 13
#define SWC 13
#define SW2 SW
#define NSW 27
#define NA0 3
#define DA 6
#define NQ 30
#define CTOT 9
#define EARLY 5
static_assert(NQ >= 24 && NA0 <= DA && DA <= 11 && CTOT < LAG, "every load of the stream is issued exactly once, before its use");
constexpr int due_ns(int n) { return TOFF + (n <= 50 ? TAU * n : TAU * 50 + TAUC * (n - 50)) + (n < 48 ? 0 : 40); }

constexpr int WS_EPOCH = 0;
constexpr int WS_Q = 4096;
constexpr int WS_G = WS_Q + 32 * 5120;
constexpr int WS_O = WS_G + 24 * 5120;
constexpr int WS_M = WS_O + 256 * 1536;
constexpr int WS_B = WS_M + 256 * 128;
constexpr int WS_S = WS_B + 24 * 5120;
constexpr int WS_HQ = WS_S + 160 * 1024;
constexpr int WS_HO = WS_HQ + 32 * 128;
constexpr int WS_END = WS_HO + 32 * 128;
constexpr int WS_STAMP = 2 << 20;

constexpr int L_RED = 0;
constexpr int L_X = 8192;
constexpr int L_Q = L_X + 2560;
constexpr int L_H = L_Q + 384;
constexpr int L_M = L_H + 256;
constexpr int L_TAG = L_M + 32;
constexpr int L_F = L_M + 64;
constexpr int L_RB = L_F + 32;
constexpr int L_RS = L_RB + 2048;
constexpr int L_DUMP = L_RS + 1280;
constexpr int LDS_BYTES = 88 * 1024;

struct Params {
  const unsigned char* wq; const unsigned char* wg; const unsigned char* wu; const unsigned char* k; const unsigned char* v;
  const unsigned char* wo; const unsigned char* wd; const unsigned short* x; unsigned short* out; unsigned char* ws;
};

__device__ __forceinline__ rsrc_t mk(const void* p, int bytes) { return __builtin_amdgcn_make_buffer_rsrc((void*)p, 0, bytes, 0x00020000); }
__device__ __forceinline__ unsigned pk(float lo, float hi) { bf16x2 v = {(__bf16)lo, (__bf16)hi}; return __builtin_bit_cast(unsigned, v); }
__device__ __forceinline__ float lo16(unsigned a) { return __builtin_bit_cast(float, a << 16); }
__device__ __forceinline__ float hi16(unsigned a) { return __builtin_bit_cast(float, a & 0xffff0000u); }
__device__ __forceinline__ float rbf(float a) { return lo16(pk(a, a)); }
__device__ __forceinline__ float asf(unsigned a) { return __builtin_bit_cast(float, a); }
__device__ __forceinline__ unsigned asu(float a) { return __builtin_bit_cast(unsigned, a); }
template <int CTRL> __device__ __forceinline__ float dpp(float x) {
  return __builtin_bit_cast(float, __builtin_amdgcn_mov_dpp(__builtin_bit_cast(int, x), CTRL, 0xf, 0xf, true));
}
constexpr int XOR1 = 0xB1, XOR2 = 0x4E, XOR7 = 0x141, ROR8 = 0x128, ROR4 = 0x124, BCAST = 0x150;
__device__ __forceinline__ float sum8(float x) { x += dpp<XOR1>(x); x += dpp<XOR2>(x); x += dpp<XOR7>(x); return x; }
__device__ __forceinline__ float max8(float x) { x = fmaxf(x, dpp<XOR1>(x)); x = fmaxf(x, dpp<XOR2>(x)); x = fmaxf(x, dpp<XOR7>(x)); return x; }
__device__ __forceinline__ float sum_x16(float x) { auto s = __builtin_amdgcn_permlane16_swap(asu(x), asu(x), false, false); return asf(s[0]) + asf(s[1]); }
__device__ __forceinline__ float sum_x32(float x) { auto s = __builtin_amdgcn_permlane32_swap(asu(x), asu(x), false, false); return asf(s[0]) + asf(s[1]); }
__device__ __forceinline__ float fold32(float a, float b) { auto s = __builtin_amdgcn_permlane32_swap(asu(a), asu(b), false, false); return asf(s[0]) + asf(s[1]); }
__device__ __forceinline__ float max_x16(float x) { auto s = __builtin_amdgcn_permlane16_swap(asu(x), asu(x), false, false); return fmaxf(asf(s[0]), asf(s[1])); }
__device__ __forceinline__ float max_x32(float x) { auto s = __builtin_amdgcn_permlane32_swap(asu(x), asu(x), false, false); return fmaxf(asf(s[0]), asf(s[1])); }
__device__ __forceinline__ float sum_groups(float x) { x += dpp<ROR8>(x); return sum_x32(sum_x16(x)); }
__device__ __forceinline__ float dot2(unsigned a, unsigned b, float c) {
  return __builtin_amdgcn_fdot2_f32_bf16(__builtin_bit_cast(bf16x2, a), __builtin_bit_cast(bf16x2, b), c, false);
}
__device__ __forceinline__ f32x4 gemv(unsigned pp, v4u mask, v4u b, f32x4 c) {
  const v4u a = {pp & mask.x, pp & mask.y, pp & mask.z, pp & mask.w};
  return __builtin_amdgcn_mfma_f32_16x16x32_bf16(__builtin_bit_cast(bf16x8, a), __builtin_bit_cast(bf16x8, b), c, 0, 0, 0);
}
template <class Fn, int... I> __device__ __forceinline__ void sfor_(Fn&& f, std::integer_sequence<int, I...>) { (f(std::integral_constant<int, I>{}), ...); }
template <int N, class Fn> __device__ __forceinline__ void sfor(Fn&& f) { sfor_(f, std::make_integer_sequence<int, N>{}); }
#define INL __attribute__((always_inline))
#define IC(n) std::integral_constant<int, (n)>{}
#define RELOAD() asm volatile("" ::: "memory")
#define LD4(rs, vo, so, aux) __builtin_amdgcn_raw_buffer_load_b128(rs, vo, so, aux)
#define PUT(a, b, vo) __builtin_amdgcn_raw_buffer_store_b128(v4u{tlo, asu(a), thi, asu(b)}, rws, vo, 0, SC1)
#define HINT(off) __builtin_amdgcn_raw_buffer_store_b32(tlo, rws, lane == 0 ? (off) : OOB, 0, SC1)
#define STALE(g) (((g).x ^ tlo) | ((g).z ^ thi))
#define CONFIRM(g, off, live) do { if (__builtin_expect(__any((live) && STALE(g) != 0), 0)) { _Pragma("nounroll") \
    for (unsigned spins = 0; spins < SPIN_CAP; ++spins) { \
      asm volatile("buffer_load_dwordx4 %0, %1, %2, 0 offen sc1\n\ts_waitcnt vmcnt(0)" : "+v"(g) : "v"(off), "s"(rws) : "memory"); \
      if (!__any((live) && STALE(g) != 0)) break; } } } while (0)
#define WAIT_UNTIL(t0, ns) asm volatile("1:\n\ts_memrealtime vcc\n\ts_waitcnt lgkmcnt(0)\n\ts_sub_u32 vcc_lo, vcc_lo, %0\n\t" \
    "s_cmpk_lt_u32 vcc_lo, %1\n\ts_cbranch_scc1 1b" :: "s"(t0), "n"((ns) / 10) : "vcc", "scc", "memory")
#define PIN() __builtin_amdgcn_sched_barrier(0)
#define BARRIER() do { PIN(); asm volatile("s_waitcnt lgkmcnt(0)\n\ts_barrier" ::: "memory"); PIN(); } while (0)
#define STAMP(i) do { } while (0)

template <int N> __device__ __forceinline__ void wait_hint(const unsigned char* ip, unsigned t) {
  typedef u64 v8l __attribute__((ext_vector_type(8)));
  const u64 tt = ((u64)t << 32) | t;
#pragma nounroll
  for (int spins = 0; spins < 256; ++spins) {
    v8l v;
    asm volatile("s_load_dwordx16 %0, %1, 0x0 glc\n\ts_waitcnt lgkmcnt(0)" : "=&s"(v) : "s"(ip) : "memory");
    u64 bad = 0;
#pragma unroll
    for (int i = 0; i < N / 2; ++i) bad |= v[i] ^ tt;
    asm volatile("" : "+s"(bad), "+s"(spins));
    if (bad == 0) break;
  }
}

__global__ void __launch_bounds__(320, 2) __attribute__((aligned(4096))) fused_decode(const unsigned char* a_wq, const unsigned char* a_wg, const unsigned char* a_wu, const unsigned short* a_x,
    unsigned char* a_ws, const unsigned char* a_k, const unsigned char* a_v, const unsigned char* a_wo, const unsigned char* a_wd, unsigned short* a_out) {
  __shared__ __attribute__((aligned(16))) unsigned char smem[LDS_BYTES];
  LAS unsigned char* L = (LAS unsigned char*)smem;
  STAMP(0);
  v16u ka; v4u kb;
  asm volatile("s_load_dwordx16 %0, %2, 0x0\n\ts_load_dwordx4 %1, %2, 0x40" : "=&s"(ka), "=&s"(kb) : "s"(__builtin_amdgcn_kernarg_segment_ptr()));
  const int b = blockIdx.x, tid = threadIdx.x, lane = tid & 63, w = __builtin_amdgcn_readfirstlane(tid >> 6);
  unsigned pf0, pf1;
  {
    const rsrc_t rc = mk((const void*)(__builtin_amdgcn_s_getpc() & ~127ull), CODE_BYTES);
    const int po = (tid ^ 256) * 128;
    pf0 = __builtin_amdgcn_raw_buffer_load_b32(rc, po, 0, 0);
    pf1 = __builtin_amdgcn_raw_buffer_load_b32(rc, po + 8192, 0, 0);
  }
  if (w == 4) __builtin_amdgcn_s_setprio(3);
  PIN();
  const int idx = b >> 3, h = 4 * (b & 7) + (idx & 3), s = idx >> 2;
  const int f3 = (idx * 26) >> 8, cf = 3 * (b & 7) + f3, fj = idx - 10 * f3, fo = idx < 30 ? 0 : OOB;
  const int g8 = lane >> 3, c8 = lane & 7, g16 = lane >> 4, c16 = lane & 15, hi = lane >> 5, l32 = lane & 31;
  const int dpart = 2 * w + (g8 & 1);
  const int R = 240 * s - (s & 4) * 4 + 32 * dpart + 8 * g16, cb = (R >= 640) + (R >= 1280), rq0 = R - 640 * cb;
  const int rf0 = 128 * (fj - 5 * (fj >= 5)) + 32 * w + 4 * g8;
  const bool rok = (unsigned)(R - 240 * s) < 240u;
  int vq = (rq0 * 12288 + h * 384 + cb * 128 + c8 * 16) | (rok ? 0 : OOB), vf = (rf0 * 3072 + cf * 128 + c8 * 16) | fo;
  int vkv = (h * SK + (SK - WIN) + 32 * s + 8 * w + g8) * 384 + c8 * 16;
  int vx = lane * 20;
  v4u xa;
  v2u ep;
  unsigned xb, t_own;
  int role = w;
  asm volatile("" : "+v"(vq), "+v"(vf), "+v"(vkv), "+v"(vx), "=v"(xa), "=v"(xb), "=v"(ep));
  asm volatile("" : "=s"(t_own), "+s"(role));
  PIN();
  asm volatile("s_waitcnt lgkmcnt(0)" : "+s"(ka), "+s"(kb) :: "memory");
#define ARG(v, i) (unsigned char*)(((u64)(v)[2 * (i) + 1] << 32) | (v)[2 * (i)])
  const Params p = {ARG(ka, 0), ARG(ka, 1), ARG(ka, 2), ARG(ka, 5), ARG(ka, 6), ARG(ka, 7), ARG(kb, 0), (const unsigned short*)ARG(ka, 3), (unsigned short*)ARG(kb, 1), ARG(ka, 4)};
  if (w == 4) {
    const rsrc_t rx = mk(p.x, D * 2);
    xa = LD4(rx, vx, 0, 0);
    xb = __builtin_amdgcn_raw_buffer_load_b32(rx, vx + 16, 0, 0);
    ep = __builtin_amdgcn_raw_buffer_load_b64(mk(p.ws, WS_END), WS_EPOCH, 0, SC1);
    u64 t;
    asm volatile("s_memrealtime %0\n\ts_waitcnt lgkmcnt(0)" : "=s"(t) :: "memory");
    t_own = (unsigned)t;
  }

  if (role == 4) {
    const rsrc_t rws = mk(p.ws, WS_END), rx = mk(p.x, D * 2);
    const int rj = 16 * ((b & 7) + (idx >> 4) * 8) + (idx & 15);
    const unsigned short xo = __builtin_amdgcn_raw_buffer_load_b16(rx, (4 * rj + 2 * (lane & 1) + hi) * 2, 0, 0);
    {
      float xv[10] = {lo16(xa.x), hi16(xa.x), lo16(xa.y), hi16(xa.y), lo16(xa.z), hi16(xa.z), lo16(xa.w), hi16(xa.w), lo16(xb), hi16(xb)};
      float sm = 0.f;
#pragma unroll
      for (int i = 0; i < 10; ++i) sm += xv[i];
      const float mean = sum_groups(sum8(sm)) * (1.f / D);
      float sv = 0.f;
#pragma unroll
      for (int i = 0; i < 10; ++i) { const float d = xv[i] - mean; sv = fmaf(d, d, sv); }
      const float var = sum_groups(sum8(sv)) * (1.f / D);
      const float mb = rbf(mean), sd = rbf(__builtin_amdgcn_sqrtf(rbf(rbf(var) + 1e-5f))), rs = __builtin_amdgcn_rcpf(sd);
      unsigned xp[10];
#pragma unroll
      for (int i = 0; i < 10; ++i) { const float d = rbf(xv[i] - mb), q = d * rs, r = fmaf(fmaf(-q, sd, d), rs, q); xp[i] = pk(r, r); }
#pragma unroll
      for (int i = 0; i < 5; ++i) *(LAS v2u*)(L + L_X + lane * 40 + i * 8) = v2u{xp[2 * i], xp[2 * i + 1]};
    }
    STAMP(1);
    BARRIER();
    const u64 tag = (((u64)__builtin_amdgcn_readfirstlane(ep.y) << 32) | (unsigned)__builtin_amdgcn_readfirstlane(ep.x)) + 0x9E3779B97F4A7C15ull;
    const unsigned tlo = (unsigned)tag, thi = (unsigned)(tag >> 32);
    *(LAS v2u*)(L + L_TAG) = v2u{tlo, thi};
    if (lane < 8) *(LAS float*)(L + L_M + lane * 4) = -1.f;
    asm volatile("" :: "v"(pf0), "v"(pf1));
    const int nd = s == 2 ? 5 : s == 5 ? 3 : 8;
    int q_off = (WS_Q + h * 5120 + (s + (3 * s >> 3)) * 512 + lane * 16) | (hi == 0 || nd < 8 ? 0 : OOB);
    int g_off = (WS_G + cf * 5120 + fj * 512 + lane * 16) | (hi == 0 ? fo : OOB);
    int hq_off = lane == 0 ? WS_HQ + h * 128 + s * 4 : OOB;
    asm volatile("" : "+v"(q_off), "+v"(g_off), "+v"(hq_off));
    STAMP(7);
    BARRIER();
    STAMP(2);
    {
      f32x2 t[8], c[8];
#pragma unroll
      for (int i = 0; i < 8; ++i) { t[i] = *(LAS f32x2*)(L + L_RED + i * 512 + l32 * 8); c[i] = *(LAS f32x2*)(L + L_RED + i * 512 + 256 + l32 * 8); }
      f32x2 s1 = t[0] + t[1] + t[2], s2 = t[3] + t[4], s3 = t[5] + t[6] + t[7];
      asm volatile("" : "+v"(s1), "+v"(s2), "+v"(s3));
      const f32x2 z = {0.f, 0.f};
      const f32x2 a = (hi ? z : s1) + ((nd == 3) == (hi != 0) ? s2 : z) + ((nd == 8) == (hi == 0) ? s3 : z);
      PUT(a.x, a.y, q_off);
      __builtin_amdgcn_raw_buffer_store_b32(tlo, rws, hq_off, 0, SC1);
      const f32x2 g = ((c[0] + c[1]) + (c[2] + c[3])) + ((c[4] + c[5]) + (c[6] + c[7]));
      PUT(g.x, g.y, g_off);
    }
    STAMP(3);
    {
      v4u r[5];
      wait_hint<8>(p.ws + WS_HQ + h * 128, tlo);
      STAMP(17);
#pragma nounroll
      for (unsigned spins = 0; spins < SPIN_CAP; ++spins) {
        RELOAD();
#pragma unroll
        for (int i = 0; i < 5; ++i) r[i] = LD4(rws, WS_Q + h * 5120 + lane * 16, i * 1024, SC1);
        if (!__any((STALE(r[0]) | STALE(r[1]) | STALE(r[2]) | STALE(r[3]) | STALE(r[4])) != 0)) break;
      }
      STAMP(4);
      const float a0 = asf(r[0].y) + (hi ? 0.f : asf(r[1].y)), a1 = asf(r[0].w) + (hi ? 0.f : asf(r[1].w));
      const float b0 = asf(r[2].y) + asf(hi ? r[1].y : r[3].y), b1 = asf(r[2].w) + asf(hi ? r[1].w : r[3].w);
      const float c0 = asf(r[4].y) + (hi ? asf(r[3].y) : 0.f), c1 = asf(r[4].w) + (hi ? asf(r[3].w) : 0.f);
      *(LAS unsigned*)(L + L_Q + lane * 4) = pk(fold32(a0, b0), fold32(a1, b1));
      *(LAS unsigned*)(L + L_Q + 256 + l32 * 4) = pk(sum_x32(c0), sum_x32(c1));
    }
    STAMP(5);
    BARRIER();
    if (idx < 30) {
      v4u r[5];
#pragma nounroll
      for (unsigned spins = 0; spins < SPIN_CAP; ++spins) {
        RELOAD();
#pragma unroll
        for (int i = 0; i < 5; ++i) r[i] = LD4(rws, WS_G + cf * 5120 + lane * 16, i * 1024, SC1);
        if (!__any((STALE(r[0]) | STALE(r[1]) | STALE(r[2]) | STALE(r[3]) | STALE(r[4])) != 0)) break;
      }
      const float g0 = sum_x32(asf(r[0].y) + asf(r[1].y) + (hi ? 0.f : asf(r[2].y))), g1 = sum_x32(asf(r[0].w) + asf(r[1].w) + (hi ? 0.f : asf(r[2].w)));
      const float u0 = sum_x32(asf(r[3].y) + asf(r[4].y) + (hi ? asf(r[2].y) : 0.f)), u1 = sum_x32(asf(r[3].w) + asf(r[4].w) + (hi ? asf(r[2].w) : 0.f));
      const float h0 = rbf(g0) * rbf(u0), h1 = rbf(g1) * rbf(u1);
      *(LAS v2u*)(L + L_H + l32 * 8) = v2u{pk(h0, h0), pk(h1, h1)};
    } else {
      *(LAS v2u*)(L + L_H + l32 * 8) = v2u{0u, 0u};
    }
    STAMP(6);
    float wt[4];
    {
      f32x4 a, c;
#pragma nounroll
      for (unsigned spins = 0; spins < (SPIN_CAP << 4); ++spins) {
        RELOAD();
        a = *(LAS f32x4*)(L + L_M); c = *(LAS f32x4*)(L + L_M + 16);
        if (!__any(a.y < 0.f || a.w < 0.f || c.y < 0.f || c.w < 0.f)) break;
        __builtin_amdgcn_s_sleep(2);
      }
      const float mx = fmaxf(fmaxf(a.x, a.z), fmaxf(c.x, c.z));
      wt[0] = __builtin_amdgcn_exp2f((a.x - mx) * LOG2E); wt[1] = __builtin_amdgcn_exp2f((a.z - mx) * LOG2E);
      wt[2] = __builtin_amdgcn_exp2f((c.x - mx) * LOG2E); wt[3] = __builtin_amdgcn_exp2f((c.z - mx) * LOG2E);
      PUT(mx, (wt[0] * a.y + wt[1] * a.w) + (wt[2] * c.y + wt[3] * c.w), lane == 0 ? WS_M + (h * 8 + s) * 128 : OOB);
    }
    int o_off = WS_O + (h * 8 + s) * 1536 + lane * 16, o_of1 = (o_off + 1024) | (hi ? OOB : 0);
    asm volatile("" : "+v"(o_off), "+v"(o_of1), "+v"(wt[0]), "+v"(wt[1]), "+v"(wt[2]), "+v"(wt[3]));
    STAMP(8);
    BARRIER();
    HINT(WS_HO + h * 128 + s * 4);
    const int moff = (WS_M + (h * 8 + c8) * 128) | (lane < 8 ? 0 : OOB);
    v4u ml = LD4(rws, moff, 0, SC1);
    STAMP(9);
    BARRIER();
    {
      f32x2 t[8], u[8];
#pragma unroll
      for (int i = 0; i < 8; ++i) t[i] = *(LAS f32x2*)(L + L_RED + i * 1024 + lane * 8);
#pragma unroll
      for (int i = 0; i < 8; ++i) u[i] = *(LAS f32x2*)(L + L_RED + i * 1024 + 512 + l32 * 8);
      PIN();
      const f32x2 a = (wt[0] * (t[0] + t[1]) + wt[1] * (t[2] + t[3])) + (wt[2] * (t[4] + t[5]) + wt[3] * (t[6] + t[7]));
      const f32x2 c = (wt[0] * (u[0] + u[1]) + wt[1] * (u[2] + u[3])) + (wt[2] * (u[4] + u[5]) + wt[3] * (u[6] + u[7]));
      PUT(a.x, a.y, o_off);
      PUT(c.x, c.y, o_of1);
    }
    STAMP(10);
    BARRIER();
    STAMP(16);
    wait_hint<8>(p.ws + WS_HO + h * 128, tlo);
    STAMP(22);
    asm volatile("s_waitcnt vmcnt(0)" ::: "memory");
    STAMP(12);
    BARRIER();
    {
      CONFIRM(ml, moff, lane < 8);
      const float M = max8(asf(ml.y));
      const float e = __builtin_amdgcn_exp2f((asf(ml.y) - M) * LOG2E);
      const float f = e * __builtin_amdgcn_rcpf(sum8(e * asf(ml.w)));
      if (lane < 8) *(LAS float*)(L + L_F + lane * 4) = f;
    }
    STAMP(11);
    BARRIER();
    {
      f32x2 a[8];
#pragma unroll
      for (int i = 0; i < 8; ++i) a[i] = *(LAS f32x2*)(L + L_RB + i * 256 + l32 * 8);
      const f32x2 t = ((a[0] + a[1]) + (a[2] + a[3])) + ((a[4] + a[5]) + (a[6] + a[7]));
      PUT(t.x, t.y, (WS_B + cf * 5120 + fj * 512 + lane * 16) | (hi == 0 ? fo : OOB));
    }
    const int boff = (WS_B + (lane >> 1) * 5120 + rj * 32 + (lane & 1) * 16) | (lane < 48 ? 0 : OOB);
    BARRIER();
    if (rj < 160) {
      v4u r = LD4(rws, boff, 0, SC1);
      CONFIRM(r, boff, lane < 48);
      float t = fold32(asf(r.y), asf(r.w));
      t += dpp<XOR2>(t); t += dpp<ROR4>(t); t += dpp<ROR8>(t); t = sum_x16(t);
      const float bv = rbf(t);
      float gate = rbf(bv / (1.f + __expf(-bv))), xv = lo16(xo);
      int oo = l32 < 2 ? (4 * rj + 2 * (lane & 1) + hi) * 2 : OOB;
      int r_off = WS_S + rj * 1024 + lane * 16;
      rsrc_t rout = mk(p.out, D * 2);
      asm volatile("" : "+v"(gate), "+v"(xv), "+v"(oo), "+v"(r_off));
      asm volatile("" : "+s"(rout));
      STAMP(19);
      WAIT_UNTIL(t_own, due_ns(61) + TFIN);
      {
        unsigned left = SPIN_CAP;
        u64 tmp;
        asm volatile("1:\n\tbuffer_load_dwordx4 v[40:43], %3, %4, 0 offen sc1\n\ts_sub_u32 %1, %1, 1\n\ts_waitcnt vmcnt(0)\n\ts_cbranch_scc1 2f\n\t"
                     "v_cmp_ne_u32 vcc, %5, v40\n\tv_cmp_ne_u32 %2, %6, v42\n\ts_or_b64 vcc, vcc, %2\n\ts_cbranch_scc1 1b\n2:"
                     : "=&{v[40:43]}"(r), "+s"(left), "=&s"(tmp) : "v"(r_off), "s"(rws), "s"(tlo), "s"(thi) : "vcc", "scc", "memory");
      }
      STAMP(14);
      t = fold32(asf(r.y), asf(r.w));
      t += dpp<XOR2>(t); t += dpp<ROR4>(t); t += dpp<ROR8>(t); t = sum_x16(t);
      const float res = xv + rbf(rbf(t) * gate);
      __builtin_amdgcn_raw_buffer_store_b16((unsigned short)pk(res, res), rout, oo, 0, 0);
      if (b == 0) __builtin_amdgcn_raw_buffer_store_b64(v2u{tlo, thi}, rws, lane == 0 ? WS_EPOCH : OOB, 0, SC1);
    }
  } else {
    STAMP(17);
    v4u wa[12];
    u64 t_entry;
    asm volatile("s_memrealtime %0" : "=s"(t_entry) :: "memory");
    PIN();
    {
      const rsrc_t rq = mk(p.wq, D * H * DH * 2);
      sfor<12>([&](auto I) INL {
        constexpr int i = decltype(I)::value;
        if constexpr (i == NAB) asm volatile("s_waitcnt lgkmcnt(0)" : "+s"(t_entry) :: "memory");
        if constexpr (i >= NAB)
          WAIT_UNTIL((unsigned)t_entry, TA0 + (i - NAB) * TA);
        PIN();
        if constexpr (i < 8) wa[i] = LD4(rq, vq, i * 12288, NT); else wa[i] = LD4(mk(fj < 5 ? p.wg : p.wu, D * F * 2), vf, (i - 8) * 3072, NT);
        PIN();
      });
    }
    const rsrc_t rk = mk(p.k, H * SK * DH * 2), rv = mk(p.v, H * SK * DH * 2), ro = mk(p.wo, H * DH * D * 2), rd = mk(p.wd, F * D * 2);
    const int vo = ((192 * h + 48 * w + 8 * (g16 & 1) + (g16 >> 1)) * 1280 + (10 * s + c16) * 16) | (c16 < 10 ? 0 : OOB);
    const int vdn = ((64 * cf + 16 * w + 2 * g8) * 1280 + fj * 128 + c8 * 16) | fo;
    const unsigned t_start = (unsigned)t_entry, t_stag = t_start + (h & 3) * STAG;
    v4u kd[24], vd[24], wc[14];
    auto issue = [&](auto N) INL {
      constexpr int n = decltype(N)::value;
      if constexpr (n < 24) kd[n] = LD4(rk, vkv + (n % 3) * 128, ((n / 3 + h) & 7) * 98304, NT);
      else if constexpr (n < 48) vd[n - 24] = LD4(rv, vkv + (n % 3) * 128, ((n / 3 + h) & 7) * 98304, NT);
      else if constexpr (n < 50) wc[n - 48] = LD4(rd, vdn, (n - 48) * 1280, NT);
      else wc[n - 48] = LD4(ro, vo, (16 * ((n - 50) >> 2) + 2 * ((n - 50) & 3)) * 1280, NT);
      PIN();
    };
    auto paced = [&](auto N) INL {
      constexpr int n = decltype(N)::value;
      if constexpr (n >= 48 || n % 3 == 0) {
        asm volatile("s_waitcnt vmcnt(%0)" :: "n"(n < 48 ? (n < NSW ? SW : SW2) - 3 : SWC - 1) : "memory");
        WAIT_UNTIL(n < 48 ? t_stag : t_start, due_ns(n));
      }
      PIN();
      issue(N);
    };
#define ISSUE(lo, hi, fn) sfor<(hi) - (lo)>([&](auto I_) INL { fn(IC((lo) + decltype(I_)::value)); })
    v4u mask;
    {
      const unsigned hm = (lane & 1) ? 0xffff0000u : 0x0000ffffu;
      const int dsel = c8 >> 1;
      mask = v4u{dsel == 0 ? hm : 0u, dsel == 1 ? hm : 0u, dsel == 2 ? hm : 0u, dsel == 3 ? hm : 0u};
    }
    asm volatile("" :: "v"(pf0), "v"(pf1));
    ISSUE(0, NA0, paced);
    STAMP(1);
    BARRIER();
    v4u xn[3];
    xn[0] = *(LAS v4u*)(L + L_X + (rok ? rq0 : 0) * 4); xn[1] = *(LAS v4u*)(L + L_X + (rok ? rq0 : 0) * 4 + 16); xn[2] = *(LAS v4u*)(L + L_X + rf0 * 4);
    f32x4 qa = {0.f, 0.f, 0.f, 0.f}, fa = {0.f, 0.f, 0.f, 0.f};
    sfor<12>([&](auto J) INL {
      constexpr int j = decltype(J)::value;
      if constexpr (j < 8) qa = gemv(xn[j / 4][j % 4], mask, wa[j], qa); else fa = gemv(xn[2][j % 4], mask, wa[j], fa);
      PIN();
      if constexpr (j >= 11 - DA + NA0 && j < 11) paced(IC(j - (11 - DA)));
    });
    const bool dvalid = hi == (g8 & 1);
    const int d4 = 2 * c8 + (g16 & 1);
    if (dvalid) { *(LAS f32x4*)(L + L_RED + dpart * 512 + d4 * 16) = qa; *(LAS f32x4*)(L + L_RED + dpart * 512 + 256 + d4 * 16) = fa; }
    STAMP(2);
    BARRIER();
    ISSUE(DA, NQ, paced);
    STAMP(5);
    BARRIER();
    STAMP(6);
    constexpr int N1 = NQ, N2 = NQ + 7;
    v4u qf[3];
#pragma unroll
    for (int t = 0; t < 3; ++t) qf[t] = *(LAS v4u*)(L + L_Q + t * 128 + c8 * 16);
    const v2u tg = *(LAS v2u*)(L + L_TAG);
    const unsigned tlo = tg.x, thi = tg.y;
    float sc[8];
#pragma unroll
    for (int i = 0; i < 8; ++i) sc[i] = 0.f;
    sfor<3>([&](auto T) INL {
      constexpr int t = decltype(T)::value;
#pragma unroll
      for (int e = 0; e < 4; ++e) {
#pragma unroll
        for (int i = 0; i < 8; ++i) sc[i] = dot2(kd[3 * i + t][e], qf[t][e], sc[i]);
        PIN();
      }
      if constexpr (t > 0) paced(IC(N1 + t - 1));
    });
    sfor<2>([&](auto T) INL {
      constexpr int t = decltype(T)::value;
#pragma unroll
      for (int i = 4 * t; i < 4 * t + 4; ++i) { sc[i] = rbf(rbf(sum8(sc[i])) * 0.07216878364870322f); asm volatile("" : "+v"(sc[i])); }
      PIN();
      paced(IC(N1 + 2 + t));
    });
    float mx = -3.0e38f;
#pragma unroll
    for (int i = 0; i < 8; ++i) mx = fmaxf(mx, sc[i]);
    mx = fmaxf(mx, dpp<ROR8>(mx)); mx = max_x32(max_x16(mx));
    asm volatile("" : "+v"(mx));
    PIN();
    paced(IC(N1 + 4));
    v4u pop[8];
    float lsum = 0.f;
    sfor<2>([&](auto T) INL {
      constexpr int t = decltype(T)::value;
#pragma unroll
      for (int i = 4 * t; i < 4 * t + 4; ++i) {
        const float e = __builtin_amdgcn_exp2f((sc[i] - mx) * LOG2E);
        lsum += e;
        const unsigned pp = pk(e, e);
        pop[i] = v4u{pp & mask.x, pp & mask.y, pp & mask.z, pp & mask.w};
        asm volatile("" : "+v"(pop[i]));
      }
      PIN();
      paced(IC(N1 + 5 + t));
    });
    lsum = sum_groups(lsum);
    if (lane == 0) *(LAS f32x2*)(L + L_M + w * 8) = f32x2{mx, lsum};
    PIN();
    STAMP(8);
    f32x4 oa[3];
#pragma unroll
    for (int t = 0; t < 3; ++t) oa[t] = f32x4{0.f, 0.f, 0.f, 0.f};
    int la = dvalid ? L_RED + dpart * 1024 + d4 * 16 : L_DUMP + lane * 16;
    asm volatile("" : "+v"(la));
    ISSUE(N2, N2 < 24 + LAG ? 24 + LAG : N2, paced);
    sfor<24>([&](auto J) INL {
      constexpr int j = decltype(J)::value;
      oa[j % 3] = __builtin_amdgcn_mfma_f32_16x16x32_bf16(__builtin_bit_cast(bf16x8, pop[j / 3]), __builtin_bit_cast(bf16x8, vd[j]), oa[j % 3], 0, 0, 0);
      PIN();
      if constexpr (24 + j + LAG >= N2 && 24 + j + LAG < 48 + CTOT) paced(IC(24 + j + LAG));
      if constexpr (j == 23 - EARLY) BARRIER();
    });
#pragma unroll
    for (int t = 0; t < 3; ++t) *(LAS f32x4*)(L + la + t * 256) = oa[t];
    STAMP(9);
    BARRIER();
    {
      const v2u hh = *(LAS v2u*)(L + L_H + (16 * w + 2 * g8) * 4);
      const f32x4 ba = gemv(hh.y, mask, wc[1], gemv(hh.x, mask, wc[0], f32x4{0.f, 0.f, 0.f, 0.f}));
      if (dvalid) *(LAS f32x4*)(L + L_RB + dpart * 256 + d4 * 16) = ba;
    }
    BARRIER();
    ISSUE(48 + CTOT, 62, issue);
    STAMP(10);
    const rsrc_t rws = mk(p.ws, WS_END);
    int ooff = WS_O + (h * 8 + g8) * 1536 + (24 * w + c8) * 16, ls = c16 < 10 && hi == 0 ? L_RS + w * 320 + (2 * c16 + g16) * 16 : L_DUMP + lane * 16;
    int s_off = (WS_S + (20 * s + (lane >> 1)) * 1024 + h * 32 + (lane & 1) * 16) | (lane < 40 ? 0 : OOB), lr = L_RS + (lane < 40 ? lane : 0) * 8;
    asm volatile("" : "+v"(ooff), "+v"(ls), "+v"(s_off), "+v"(lr));
    v4u og[3];
    BARRIER();
    STAMP(21);
#pragma unroll
    for (int j = 0; j < 3; ++j) og[j] = LD4(rws, ooff + 128 * j, 0, SC1);
    BARRIER();
    float f = *(LAS float*)(L + L_F + g8 * 4);
    asm volatile("" : "+v"(f));
    PIN();
    unsigned bad = STALE(og[0]) | STALE(og[1]) | STALE(og[2]);
    asm volatile("" : "+v"(bad));
    if (__builtin_expect(__any(bad != 0), 0)) {
      STAMP(18);
#pragma nounroll
      for (unsigned spins = 0; spins < SPIN_CAP; ++spins) {
        asm volatile("buffer_load_dwordx4 %0, %3, %4, 0 offen sc1\n\tbuffer_load_dwordx4 %1, %3, %4, 0 offen offset:128 sc1\n\t"
                     "buffer_load_dwordx4 %2, %3, %4, 0 offen offset:256 sc1\n\ts_waitcnt vmcnt(0)"
                     : "+v"(og[0]), "+v"(og[1]), "+v"(og[2]) : "v"(ooff), "s"(rws) : "memory");
        if (!__any((STALE(og[0]) | STALE(og[1]) | STALE(og[2])) != 0)) break;
      }
    }
    STAMP(11);
    {
      float y[3], z[3];
      int x[3];
#pragma unroll
      for (int j = 0; j < 3; ++j) { y[j] = f * asf(og[j].y); z[j] = f * asf(og[j].w); }
      PIN();
#pragma unroll
      for (int j = 0; j < 3; ++j) y[j] = fold32(y[j], z[j]);
      PIN();
#pragma unroll
      for (int j = 0; j < 3; ++j) y[j] += dpp<ROR8>(y[j]);
      PIN();
#pragma unroll
      for (int j = 0; j < 3; ++j) z[j] = dpp<ROR4>(y[j]);
      PIN();
#pragma unroll
      for (int j = 0; j < 3; ++j) { const auto t = __builtin_amdgcn_permlane16_swap(asu(y[j]), asu(z[j]), false, false); y[j] = asf(t[0]); z[j] = asf(t[1]); }
      PIN();
#pragma unroll
      for (int j = 0; j < 3; ++j) y[j] += z[j];
      PIN();
#pragma unroll
      for (int j = 0; j < 3; ++j) x[j] = (int)pk(y[j], y[j]);
      PIN();
      f32x4 sa = {0.f, 0.f, 0.f, 0.f};
      v4u op[13];
      auto build = [&](auto N) INL {
        constexpr int n = decltype(N)::value;
        const unsigned pp = (unsigned)__builtin_amdgcn_mov_dpp(x[n / 4], BCAST + n % 4, 0xf, 0xf, true);
        op[n] = v4u{pp & mask.x, pp & mask.y, pp & mask.z, pp & mask.w};
        PIN();
      };
      build(IC(0));
      sfor<12>([&](auto N) INL {
        constexpr int n = decltype(N)::value;
        if constexpr (n < 11) build(IC(n + 1));
        sa = __builtin_amdgcn_mfma_f32_16x16x32_bf16(__builtin_bit_cast(bf16x8, op[n]), __builtin_bit_cast(bf16x8, wc[2 + n]), sa, 0, 0, 0);
        PIN();
      });
      *(LAS f32x4*)(L + ls) = sa;
#pragma unroll
      for (int n = 2; n < 14; ++n) asm volatile("" :: "v"(wc[n]));
    }
    STAMP(12);
    BARRIER();
    if (w == 1) {
      const f32x2 a = (*(LAS f32x2*)(L + lr) + *(LAS f32x2*)(L + lr + 320)) + (*(LAS f32x2*)(L + lr + 640) + *(LAS f32x2*)(L + lr + 960));
      PUT(a.x, a.y, s_off);
      STAMP(13);
    }
  }
}
}

extern "C" void kernel_launch(void* const* d_in, const int* in_sizes, int n_in, void* d_out, int out_size, void* d_ws, size_t ws_size,
                              hipStream_t stream) {
  Params p;
  p.x = (const unsigned short*)d_in[0];
  p.k = (const unsigned char*)d_in[1];
  p.v = (const unsigned char*)d_in[2];
  p.wq = (const unsigned char*)d_in[3];
  p.wo = (const unsigned char*)d_in[4];
  p.wg = (const unsigned char*)d_in[6];
  p.wu = (const unsigned char*)d_in[7];
  p.wd = (const unsigned char*)d_in[8];
  p.out = (unsigned short*)d_out;
  p.ws = (unsigned char*)d_ws;
  hipLaunchKernelGGL(fused_decode, dim3(256), dim3(320), 0, stream, p.wq, p.wg, p.wu, p.x, p.ws, p.k, p.v, p.wo, p.wd, p.out);
}
```

```cpp
#include <hip/hip_runtime.h>
#include <hip/hip_bf16.h>
#include <utility>

namespace {
typedef unsigned v4u __attribute__((ext_vector_type(4)));
typedef unsigned v2u __attribute__((ext_vector_type(2)));
typedef unsigned v16u __attribute__((ext_vector_type(16)));
typedef float f32x4 __attribute__((ext_vector_type(4)));
typedef float f32x2 __attribute__((ext_vector_type(2)));
typedef __bf16 bf16x8 __attribute__((ext_vector_type(8)));
typedef __bf16 bf16x2 __attribute__((ext_vector_type(2)));
typedef unsigned long long u64;
typedef __amdgpu_buffer_rsrc_t rsrc_t;
#define LAS __attribute__((address_space(3)))

constexpr int D = 640, H = 32, DH = 192, SK = 8192, WIN = 2048, F = 1536;
constexpr int NT = 2, SC1 = 16;
constexpr int OOB = 0x40000000;
constexpr unsigned SPIN_CAP = 1u << 16;
constexpr float LOG2E = 1.4426950408889634f;
constexpr int CODE_BYTES = 10752;

constexpr int TAU0 = 152, TAU1 = 142, TAU2 = 156;
constexpr int NT1 = 21, NT2 = 36;
constexpr int TAUC = 85;
constexpr int NAB = 6, TA0 = 400, TA = 125;
constexpr int TOFF = 1200;
constexpr int STAG = 11;
constexpr int TFIN = 1750;
constexpr int SW = 13;
constexpr int NA0 = 3, DA = 6;
constexpr int NQ = 30;
constexpr int CTOT = 9;
constexpr int EARLY = 3;
static_assert(NQ >= 24 && NA0 <= DA && DA <= 11 && CTOT < SW && NQ + 7 >= 24 + SW, "every load of the stream is issued exactly once, before its use");
constexpr int due_ns(int n) { return n == 0 ? TOFF : due_ns(n - 1) + (n <= NT1 ? TAU0 : n <= NT2 ? TAU1 : n <= 50 ? TAU2 : TAUC) + (n == 48 ? 40 : 0); }

constexpr int WS_EPOCH = 0;
constexpr int WS_Q = 4096;
constexpr int WS_G = WS_Q + 32 * 5120;
constexpr int WS_O = WS_G + 24 * 5120;
constexpr int WS_M = WS_O + 256 * 1536;
constexpr int WS_B = WS_M + 256 * 128;
constexpr int WS_S = WS_B + 24 * 5120;
constexpr int WS_HQ = WS_S + 160 * 1024;
constexpr int WS_HO = WS_HQ + 32 * 128;
constexpr int WS_END = WS_HO + 32 * 128;

constexpr int L_RED = 0;
constexpr int L_X = 8192;
constexpr int L_Q = L_X + 2560;
constexpr int L_H = L_Q + 384;
constexpr int L_M = L_H + 256;
constexpr int L_TAG = L_M + 32;
constexpr int L_F = L_M + 64;
constexpr int L_RB = L_F + 32;
constexpr int L_RS = L_RB + 2048;
constexpr int L_DUMP = L_RS + 1280;
constexpr int LDS_BYTES = 88 * 1024;

struct Params {
  const unsigned char* wq; const unsigned char* wg; const unsigned char* wu; const unsigned char* k; const unsigned char* v;
  const unsigned char* wo; const unsigned char* wd; const unsigned short* x; unsigned short* out; unsigned char* ws;
};

__device__ __forceinline__ rsrc_t mk(const void* p, int bytes) { return __builtin_amdgcn_make_buffer_rsrc((void*)p, 0, bytes, 0x00020000); }
__device__ __forceinline__ unsigned pk(float lo, float hi) { bf16x2 v = {(__bf16)lo, (__bf16)hi}; return __builtin_bit_cast(unsigned, v); }
__device__ __forceinline__ float lo16(unsigned a) { return __builtin_bit_cast(float, a << 16); }
__device__ __forceinline__ float hi16(unsigned a) { return __builtin_bit_cast(float, a & 0xffff0000u); }
__device__ __forceinline__ float rbf(float a) { return lo16(pk(a, a)); }
__device__ __forceinline__ float asf(unsigned a) { return __builtin_bit_cast(float, a); }
__device__ __forceinline__ unsigned asu(float a) { return __builtin_bit_cast(unsigned, a); }
template <int CTRL> __device__ __forceinline__ float dpp(float x) {
  return __builtin_bit_cast(float, __builtin_amdgcn_mov_dpp(__builtin_bit_cast(int, x), CTRL, 0xf, 0xf, true));
}
constexpr int XOR1 = 0xB1, XOR2 = 0x4E, XOR7 = 0x141, ROR8 = 0x128, ROR4 = 0x124, BCAST = 0x150;
__device__ __forceinline__ float sum8(float x) { x += dpp<XOR1>(x); x += dpp<XOR2>(x); x += dpp<XOR7>(x); return x; }
__device__ __forceinline__ float max8(float x) { x = fmaxf(x, dpp<XOR1>(x)); x = fmaxf(x, dpp<XOR2>(x)); x = fmaxf(x, dpp<XOR7>(x)); return x; }
__device__ __forceinline__ float sum_x16(float x) { auto s = __builtin_amdgcn_permlane16_swap(asu(x), asu(x), false, false); return asf(s[0]) + asf(s[1]); }
__device__ __forceinline__ float sum_x32(float x) { auto s = __builtin_amdgcn_permlane32_swap(asu(x), asu(x), false, false); return asf(s[0]) + asf(s[1]); }
__device__ __forceinline__ float fold32(float a, float b) { auto s = __builtin_amdgcn_permlane32_swap(asu(a), asu(b), false, false); return asf(s[0]) + asf(s[1]); }
__device__ __forceinline__ float max_x16(float x) { auto s = __builtin_amdgcn_permlane16_swap(asu(x), asu(x), false, false); return fmaxf(asf(s[0]), asf(s[1])); }
__device__ __forceinline__ float max_x32(float x) { auto s = __builtin_amdgcn_permlane32_swap(asu(x), asu(x), false, false); return fmaxf(asf(s[0]), asf(s[1])); }
__device__ __forceinline__ float sum_groups(float x) { x += dpp<ROR8>(x); return sum_x32(sum_x16(x)); }
__device__ __forceinline__ float dot2(unsigned a, unsigned b, float c) {
  return __builtin_amdgcn_fdot2_f32_bf16(__builtin_bit_cast(bf16x2, a), __builtin_bit_cast(bf16x2, b), c, false);
}
__device__ __forceinline__ f32x4 gemv(unsigned pp, v4u mask, v4u b, f32x4 c) {
  const v4u a = {pp & mask.x, pp & mask.y, pp & mask.z, pp & mask.w};
  return __builtin_amdgcn_mfma_f32_16x16x32_bf16(__builtin_bit_cast(bf16x8, a), __builtin_bit_cast(bf16x8, b), c, 0, 0, 0);
}
template <class Fn, int... I> __device__ __forceinline__ void sfor_(Fn&& f, std::integer_sequence<int, I...>) { (f(std::integral_constant<int, I>{}), ...); }
template <int N, class Fn> __device__ __forceinline__ void sfor(Fn&& f) { sfor_(f, std::make_integer_sequence<int, N>{}); }
#define INL __attribute__((always_inline))
#define IC(n) std::integral_constant<int, (n)>{}
#define RELOAD() asm volatile("" ::: "memory")
#define LD4(rs, vo, so, aux) __builtin_amdgcn_raw_buffer_load_b128(rs, vo, so, aux)
#define PUT(a, b, vo) __builtin_amdgcn_raw_buffer_store_b128(v4u{tlo, asu(a), thi, asu(b)}, rws, vo, 0, SC1)
#define HINT(off) __builtin_amdgcn_raw_buffer_store_b32(tlo, rws, lane == 0 ? (off) : OOB, 0, SC1)
#define STALE(g) (((g).x ^ tlo) | ((g).z ^ thi))
#define CONFIRM(g, off, live) do { if (__builtin_expect(__any((live) && STALE(g) != 0), 0)) { _Pragma("nounroll") \
    for (unsigned spins = 0; spins < SPIN_CAP; ++spins) { \
      asm volatile("buffer_load_dwordx4 %0, %1, %2, 0 offen sc1\n\ts_waitcnt vmcnt(0)" : "+v"(g) : "v"(off), "s"(rws) : "memory"); \
      if (!__any((live) && STALE(g) != 0)) break; } } } while (0)
#define WAIT_UNTIL(t0, ns) asm volatile("1:\n\ts_memrealtime vcc\n\ts_waitcnt lgkmcnt(0)\n\ts_sub_u32 vcc_lo, vcc_lo, %0\n\t" \
    "s_cmpk_lt_u32 vcc_lo, %1\n\ts_cbranch_scc1 1b" :: "s"(t0), "n"((ns) / 10) : "vcc", "scc", "memory")
#define PIN() __builtin_amdgcn_sched_barrier(0)
#define BARRIER() do { PIN(); asm volatile("s_waitcnt lgkmcnt(0)\n\ts_barrier" ::: "memory"); PIN(); } while (0)

template <int N> __device__ __forceinline__ void wait_hint(const unsigned char* ip, unsigned t) {
  typedef u64 v8l __attribute__((ext_vector_type(8)));
  const u64 tt = ((u64)t << 32) | t;
#pragma nounroll
  for (int spins = 0; spins < 256; ++spins) {
    v8l v;
    asm volatile("s_load_dwordx16 %0, %1, 0x0 glc\n\ts_waitcnt lgkmcnt(0)" : "=&s"(v) : "s"(ip) : "memory");
    u64 bad = 0;
#pragma unroll
    for (int i = 0; i < N / 2; ++i) bad |= v[i] ^ tt;
    asm volatile("" : "+s"(bad), "+s"(spins));
    if (bad == 0) break;
  }
}

__global__ void __launch_bounds__(320, 2) __attribute__((aligned(4096))) fused_decode(const unsigned char* a_wq, const unsigned char* a_wg, const unsigned char* a_wu, const unsigned short* a_x,
    unsigned char* a_ws, const unsigned char* a_k, const unsigned char* a_v, const unsigned char* a_wo, const unsigned char* a_wd, unsigned short* a_out) {
  __shared__ __attribute__((aligned(16))) unsigned char smem[LDS_BYTES];
  LAS unsigned char* L = (LAS unsigned char*)smem;
  v16u ka; v4u kb;
  asm volatile("s_load_dwordx16 %0, %2, 0x0\n\ts_load_dwordx4 %1, %2, 0x40" : "=&s"(ka), "=&s"(kb) : "s"(__builtin_amdgcn_kernarg_segment_ptr()));
  const int b = blockIdx.x, tid = threadIdx.x, lane = tid & 63, w = __builtin_amdgcn_readfirstlane(tid >> 6);
  unsigned pf0, pf1;
  {
    const rsrc_t rc = mk((const void*)(__builtin_amdgcn_s_getpc() & ~127ull), CODE_BYTES);
    const int po = (tid ^ 256) * 128;
    pf0 = __builtin_amdgcn_raw_buffer_load_b32(rc, po, 0, 0);
    pf1 = __builtin_amdgcn_raw_buffer_load_b32(rc, po + 8192, 0, 0);
  }
  if (w == 4) __builtin_amdgcn_s_setprio(3);
  PIN();
  const int idx = b >> 3, h = 4 * (b & 7) + (idx & 3), s = idx >> 2;
  const int f3 = (idx * 26) >> 8, cf = 3 * (b & 7) + f3, fj = idx - 10 * f3, fo = idx < 30 ? 0 : OOB;
  const int g8 = lane >> 3, c8 = lane & 7, g16 = lane >> 4, c16 = lane & 15, hi = lane >> 5, l32 = lane & 31;
  const int dpart = 2 * w + (g8 & 1);
  const int R = 240 * s - (s & 4) * 4 + 32 * dpart + 8 * g16, cb = (R >= 640) + (R >= 1280), rq0 = R - 640 * cb;
  const int rf0 = 128 * (fj - 5 * (fj >= 5)) + 32 * w + 4 * g8;
  const bool rok = (unsigned)(R - 240 * s) < 240u;
  int vq = (rq0 * 12288 + h * 384 + cb * 128 + c8 * 16) | (rok ? 0 : OOB), vf = (rf0 * 3072 + cf * 128 + c8 * 16) | fo;
  int vkv = (h * SK + (SK - WIN) + 32 * s + 8 * w + g8) * 384 + c8 * 16;
  int vx = lane * 20;
  v4u xa;
  v2u ep;
  unsigned xb, t_own;
  int role = w;
  asm volatile("" : "+v"(vq), "+v"(vf), "+v"(vkv), "+v"(vx), "=v"(xa), "=v"(xb), "=v"(ep));
  asm volatile("" : "=s"(t_own), "+s"(role));
  PIN();
  asm volatile("s_waitcnt lgkmcnt(0)" : "+s"(ka), "+s"(kb) :: "memory");
#define ARG(v, i) (unsigned char*)(((u64)(v)[2 * (i) + 1] << 32) | (v)[2 * (i)])
  const Params p = {ARG(ka, 0), ARG(ka, 1), ARG(ka, 2), ARG(ka, 5), ARG(ka, 6), ARG(ka, 7), ARG(kb, 0), (const unsigned short*)ARG(ka, 3), (unsigned short*)ARG(kb, 1), ARG(ka, 4)};
  if (w == 4) {
    const rsrc_t rx = mk(p.x, D * 2);
    xa = LD4(rx, vx, 0, 0);
    xb = __builtin_amdgcn_raw_buffer_load_b32(rx, vx + 16, 0, 0);
    ep = __builtin_amdgcn_raw_buffer_load_b64(mk(p.ws, WS_END), WS_EPOCH, 0, SC1);
    u64 t;
    asm volatile("s_memrealtime %0\n\ts_waitcnt lgkmcnt(0)" : "=s"(t) :: "memory");
    t_own = (unsigned)t;
  }

  if (role == 4) {
    const rsrc_t rws = mk(p.ws, WS_END), rx = mk(p.x, D * 2);
    const int rj = 16 * ((b & 7) + (idx >> 4) * 8) + (idx & 15);
    const unsigned short xo = __builtin_amdgcn_raw_buffer_load_b16(rx, (4 * rj + 2 * (lane & 1) + hi) * 2, 0, 0);
    {
      float xv[10] = {lo16(xa.x), hi16(xa.x), lo16(xa.y), hi16(xa.y), lo16(xa.z), hi16(xa.z), lo16(xa.w), hi16(xa.w), lo16(xb), hi16(xb)};
      float sm = 0.f;
#pragma unroll
      for (int i = 0; i < 10; ++i) sm += xv[i];
      const float mean = sum_groups(sum8(sm)) * (1.f / D);
      float sv = 0.f;
#pragma unroll
      for (int i = 0; i < 10; ++i) { const float d = xv[i] - mean; sv = fmaf(d, d, sv); }
      const float var = sum_groups(sum8(sv)) * (1.f / D);
      const float mb = rbf(mean), sd = rbf(__builtin_amdgcn_sqrtf(rbf(rbf(var) + 1e-5f))), rs = __builtin_amdgcn_rcpf(sd);
      unsigned xp[10];
#pragma unroll
      for (int i = 0; i < 10; ++i) { const float d = rbf(xv[i] - mb), q = d * rs, r = fmaf(fmaf(-q, sd, d), rs, q); xp[i] = pk(r, r); }
#pragma unroll
      for (int i = 0; i < 5; ++i) *(LAS v2u*)(L + L_X + lane * 40 + i * 8) = v2u{xp[2 * i], xp[2 * i + 1]};
    }
    BARRIER();
    const u64 tag = (((u64)__builtin_amdgcn_readfirstlane(ep.y) << 32) | (unsigned)__builtin_amdgcn_readfirstlane(ep.x)) + 0x9E3779B97F4A7C15ull;
    const unsigned tlo = (unsigned)tag, thi = (unsigned)(tag >> 32);
    *(LAS v2u*)(L + L_TAG) = v2u{tlo, thi};
    if (lane < 8) *(LAS float*)(L + L_M + lane * 4) = -1.f;
    asm volatile("" :: "v"(pf0), "v"(pf1));
    const int nd = s == 2 ? 5 : s == 5 ? 3 : 8;
    int q_off = (WS_Q + h * 5120 + (s + (3 * s >> 3)) * 512 + lane * 16) | (hi == 0 || nd < 8 ? 0 : OOB);
    int g_off = (WS_G + cf * 5120 + fj * 512 + lane * 16) | (hi == 0 ? fo : OOB);
    int hq_off = lane == 0 ? WS_HQ + h * 128 + s * 4 : OOB;
    asm volatile("" : "+v"(q_off), "+v"(g_off), "+v"(hq_off));
    BARRIER();
    {
      f32x2 t[8], c[8];
#pragma unroll
      for (int i = 0; i < 8; ++i) { t[i] = *(LAS f32x2*)(L + L_RED + i * 512 + l32 * 8); c[i] = *(LAS f32x2*)(L + L_RED + i * 512 + 256 + l32 * 8); }
      f32x2 s1 = t[0] + t[1] + t[2], s2 = t[3] + t[4], s3 = t[5] + t[6] + t[7];
      asm volatile("" : "+v"(s1), "+v"(s2), "+v"(s3));
      const f32x2 z = {0.f, 0.f};
      const f32x2 a = (hi ? z : s1) + ((nd == 3) == (hi != 0) ? s2 : z) + ((nd == 8) == (hi == 0) ? s3 : z);
      PUT(a.x, a.y, q_off);
      __builtin_amdgcn_raw_buffer_store_b32(tlo, rws, hq_off, 0, SC1);
      const f32x2 g = ((c[0] + c[1]) + (c[2] + c[3])) + ((c[4] + c[5]) + (c[6] + c[7]));
      PUT(g.x, g.y, g_off);
    }
    {
      v4u r[5];
      wait_hint<8>(p.ws + WS_HQ + h * 128, tlo);
#pragma nounroll
      for (unsigned spins = 0; spins < SPIN_CAP; ++spins) {
        RELOAD();
#pragma unroll
        for (int i = 0; i < 5; ++i) r[i] = LD4(rws, WS_Q + h * 5120 + lane * 16, i * 1024, SC1);
        if (!__any((STALE(r[0]) | STALE(r[1]) | STALE(r[2]) | STALE(r[3]) | STALE(r[4])) != 0)) break;
      }
      const float a0 = asf(r[0].y) + (hi ? 0.f : asf(r[1].y)), a1 = asf(r[0].w) + (hi ? 0.f : asf(r[1].w));
      const float b0 = asf(r[2].y) + asf(hi ? r[1].y : r[3].y), b1 = asf(r[2].w) + asf(hi ? r[1].w : r[3].w);
      const float c0 = asf(r[4].y) + (hi ? asf(r[3].y) : 0.f), c1 = asf(r[4].w) + (hi ? asf(r[3].w) : 0.f);
      *(LAS unsigned*)(L + L_Q + lane * 4) = pk(fold32(a0, b0), fold32(a1, b1));
      *(LAS unsigned*)(L + L_Q + 256 + l32 * 4) = pk(sum_x32(c0), sum_x32(c1));
    }
    BARRIER();
    if (idx < 30) {
      v4u r[5];
#pragma nounroll
      for (unsigned spins = 0; spins < SPIN_CAP; ++spins) {
        RELOAD();
#pragma unroll
        for (int i = 0; i < 5; ++i) r[i] = LD4(rws, WS_G + cf * 5120 + lane * 16, i * 1024, SC1);
        if (!__any((STALE(r[0]) | STALE(r[1]) | STALE(r[2]) | STALE(r[3]) | STALE(r[4])) != 0)) break;
      }
      const float g0 = sum_x32(asf(r[0].y) + asf(r[1].y) + (hi ? 0.f : asf(r[2].y))), g1 = sum_x32(asf(r[0].w) + asf(r[1].w) + (hi ? 0.f : asf(r[2].w)));
      const float u0 = sum_x32(asf(r[3].y) + asf(r[4].y) + (hi ? asf(r[2].y) : 0.f)), u1 = sum_x32(asf(r[3].w) + asf(r[4].w) + (hi ? asf(r[2].w) : 0.f));
      const float h0 = rbf(g0) * rbf(u0), h1 = rbf(g1) * rbf(u1);
      *(LAS v2u*)(L + L_H + l32 * 8) = v2u{pk(h0, h0), pk(h1, h1)};
    } else {
      *(LAS v2u*)(L + L_H + l32 * 8) = v2u{0u, 0u};
    }
    float wt[4];
    {
      f32x4 a, c;
#pragma nounroll
      for (unsigned spins = 0; spins < (SPIN_CAP << 4); ++spins) {
        RELOAD();
        a = *(LAS f32x4*)(L + L_M); c = *(LAS f32x4*)(L + L_M + 16);
        if (!__any(a.y < 0.f || a.w < 0.f || c.y < 0.f || c.w < 0.f)) break;
        __builtin_amdgcn_s_sleep(2);
      }
      const float mx = fmaxf(fmaxf(a.x, a.z), fmaxf(c.x, c.z));
      wt[0] = __builtin_amdgcn_exp2f((a.x - mx) * LOG2E); wt[1] = __builtin_amdgcn_exp2f((a.z - mx) * LOG2E);
      wt[2] = __builtin_amdgcn_exp2f((c.x - mx) * LOG2E); wt[3] = __builtin_amdgcn_exp2f((c.z - mx) * LOG2E);
      PUT(mx, (wt[0] * a.y + wt[1] * a.w) + (wt[2] * c.y + wt[3] * c.w), lane == 0 ? WS_M + (h * 8 + s) * 128 : OOB);
    }
    int o_off = WS_O + (h * 8 + s) * 1536 + lane * 16, o_of1 = (o_off + 1024) | (hi ? OOB : 0);
    asm volatile("" : "+v"(o_off), "+v"(o_of1), "+v"(wt[0]), "+v"(wt[1]), "+v"(wt[2]), "+v"(wt[3]));
    BARRIER();
    HINT(WS_HO + h * 128 + s * 4);
    const int moff = (WS_M + (h * 8 + c8) * 128) | (lane < 8 ? 0 : OOB);
    v4u ml = LD4(rws, moff, 0, SC1);
    BARRIER();
    {
      f32x2 t[8], u[8];
#pragma unroll
      for (int i = 0; i < 8; ++i) t[i] = *(LAS f32x2*)(L + L_RED + i * 1024 + lane * 8);
#pragma unroll
      for (int i = 0; i < 8; ++i) u[i] = *(LAS f32x2*)(L + L_RED + i * 1024 + 512 + l32 * 8);
      PIN();
      const f32x2 a = (wt[0] * (t[0] + t[1]) + wt[1] * (t[2] + t[3])) + (wt[2] * (t[4] + t[5]) + wt[3] * (t[6] + t[7]));
      const f32x2 c = (wt[0] * (u[0] + u[1]) + wt[1] * (u[2] + u[3])) + (wt[2] * (u[4] + u[5]) + wt[3] * (u[6] + u[7]));
      PUT(a.x, a.y, o_off);
      PUT(c.x, c.y, o_of1);
    }
    BARRIER();
    wait_hint<8>(p.ws + WS_HO + h * 128, tlo);
    asm volatile("s_waitcnt vmcnt(0)" ::: "memory");
    BARRIER();
    {
      CONFIRM(ml, moff, lane < 8);
      const float M = max8(asf(ml.y));
      const float e = __builtin_amdgcn_exp2f((asf(ml.y) - M) * LOG2E);
      const float f = e * __builtin_amdgcn_rcpf(sum8(e * asf(ml.w)));
      if (lane < 8) *(LAS float*)(L + L_F + lane * 4) = f;
    }
    BARRIER();
    {
      f32x2 a[8];
#pragma unroll
      for (int i = 0; i < 8; ++i) a[i] = *(LAS f32x2*)(L + L_RB + i * 256 + l32 * 8);
      const f32x2 t = ((a[0] + a[1]) + (a[2] + a[3])) + ((a[4] + a[5]) + (a[6] + a[7]));
      PUT(t.x, t.y, (WS_B + cf * 5120 + fj * 512 + lane * 16) | (hi == 0 ? fo : OOB));
    }
    const int boff = (WS_B + (lane >> 1) * 5120 + rj * 32 + (lane & 1) * 16) | (lane < 48 ? 0 : OOB);
    BARRIER();
    if (rj < 160) {
      v4u r = LD4(rws, boff, 0, SC1);
      CONFIRM(r, boff, lane < 48);
      float t = fold32(asf(r.y), asf(r.w));
      t += dpp<XOR2>(t); t += dpp<ROR4>(t); t += dpp<ROR8>(t); t = sum_x16(t);
      const float bv = rbf(t);
      float gate = rbf(bv / (1.f + __expf(-bv))), xv = lo16(xo);
      int oo = l32 < 2 ? (4 * rj + 2 * (lane & 1) + hi) * 2 : OOB;
      int r_off = WS_S + rj * 1024 + lane * 16;
      rsrc_t rout = mk(p.out, D * 2);
      asm volatile("" : "+v"(gate), "+v"(xv), "+v"(oo), "+v"(r_off));
      asm volatile("" : "+s"(rout));
      WAIT_UNTIL(t_own, due_ns(61) + TFIN);
      {
        unsigned left = SPIN_CAP;
        u64 tmp;
        asm volatile("1:\n\tbuffer_load_dwordx4 v[40:43], %3, %4, 0 offen sc1\n\ts_sub_u32 %1, %1, 1\n\ts_waitcnt vmcnt(0)\n\ts_cbranch_scc1 2f\n\t"
                     "v_cmp_ne_u32 vcc, %5, v40\n\tv_cmp_ne_u32 %2, %6, v42\n\ts_or_b64 vcc, vcc, %2\n\ts_cbranch_scc1 1b\n2:"
                     : "=&{v[40:43]}"(r), "+s"(left), "=&s"(tmp) : "v"(r_off), "s"(rws), "s"(tlo), "s"(thi) : "vcc", "scc", "memory");
      }
      t = fold32(asf(r.y), asf(r.w));
      t += dpp<XOR2>(t); t += dpp<ROR4>(t); t += dpp<ROR8>(t); t = sum_x16(t);
      const float res = xv + rbf(rbf(t) * gate);
      __builtin_amdgcn_raw_buffer_store_b16((unsigned short)pk(res, res), rout, oo, 0, 0);
      if (b == 0) __builtin_amdgcn_raw_buffer_store_b64(v2u{tlo, thi}, rws, lane == 0 ? WS_EPOCH : OOB, 0, SC1);
    }
  } else {
    v4u wa[12];
    u64 t_entry;
    asm volatile("s_memrealtime %0" : "=s"(t_entry) :: "memory");
    PIN();
    {
      const rsrc_t rq = mk(p.wq, D * H * DH * 2);
      sfor<12>([&](auto I) INL {
        constexpr int i = decltype(I)::value;
        if constexpr (i == NAB) asm volatile("s_waitcnt lgkmcnt(0)" : "+s"(t_entry) :: "memory");
        if constexpr (i >= NAB)
          WAIT_UNTIL((unsigned)t_entry, TA0 + (i - NAB) * TA);
        PIN();
        if constexpr (i < 8) wa[i] = LD4(rq, vq, i * 12288, NT); else wa[i] = LD4(mk(fj < 5 ? p.wg : p.wu, D * F * 2), vf, (i - 8) * 3072, NT);
        PIN();
      });
    }
    const rsrc_t rk = mk(p.k, H * SK * DH * 2), rv = mk(p.v, H * SK * DH * 2), ro = mk(p.wo, H * DH * D * 2), rd = mk(p.wd, F * D * 2);
    const int vo = ((192 * h + 48 * w + 8 * (g16 & 1) + (g16 >> 1)) * 1280 + (10 * s + c16) * 16) | (c16 < 10 ? 0 : OOB);
    const int vdn = ((64 * cf + 16 * w + 2 * g8) * 1280 + fj * 128 + c8 * 16) | fo;
    const unsigned t_start = (unsigned)t_entry, t_stag = t_start + (h & 3) * STAG;
    v4u kd[24], vd[24], wc[14];
    auto issue = [&](auto N) INL {
      constexpr int n = decltype(N)::value;
      if constexpr (n < 24) kd[n] = LD4(rk, vkv + (n % 3) * 128, ((n / 3 + h) & 7) * 98304, NT);
      else if constexpr (n < 48) vd[n - 24] = LD4(rv, vkv + (n % 3) * 128, ((n / 3 + h) & 7) * 98304, NT);
      else if constexpr (n < 50) wc[n - 48] = LD4(rd, vdn, (n - 48) * 1280, NT);
      else wc[n - 48] = LD4(ro, vo, (16 * ((n - 50) >> 2) + 2 * ((n - 50) & 3)) * 1280, NT);
      PIN();
    };
    auto paced = [&](auto N) INL {
      constexpr int n = decltype(N)::value;
      if constexpr (n >= 48 || n % 3 == 0) {
        asm volatile("s_waitcnt vmcnt(%0)" :: "n"(n < 48 ? SW - 3 : SW - 1) : "memory");
        WAIT_UNTIL(n < 48 ? t_stag : t_start, due_ns(n));
      }
      PIN();
      issue(N);
    };
#define ISSUE(lo, hi, fn) sfor<(hi) - (lo)>([&](auto I_) INL { fn(IC((lo) + decltype(I_)::value)); })
    v4u mask;
    {
      const unsigned hm = (lane & 1) ? 0xffff0000u : 0x0000ffffu;
      const int dsel = c8 >> 1;
      mask = v4u{dsel == 0 ? hm : 0u, dsel == 1 ? hm : 0u, dsel == 2 ? hm : 0u, dsel == 3 ? hm : 0u};
    }
    asm volatile("" :: "v"(pf0), "v"(pf1));
    ISSUE(0, NA0, paced);
    BARRIER();
    v4u xn[3];
    xn[0] = *(LAS v4u*)(L + L_X + (rok ? rq0 : 0) * 4); xn[1] = *(LAS v4u*)(L + L_X + (rok ? rq0 : 0) * 4 + 16); xn[2] = *(LAS v4u*)(L + L_X + rf0 * 4);
    f32x4 qa = {0.f, 0.f, 0.f, 0.f}, fa = {0.f, 0.f, 0.f, 0.f};
    sfor<12>([&](auto J) INL {
      constexpr int j = decltype(J)::value;
      if constexpr (j < 8) qa = gemv(xn[j / 4][j % 4], mask, wa[j], qa); else fa = gemv(xn[2][j % 4], mask, wa[j], fa);
      PIN();
      if constexpr (j >= 11 - DA + NA0 && j < 11) paced(IC(j - (11 - DA)));
    });
    const bool dvalid = hi == (g8 & 1);
    const int d4 = 2 * c8 + (g16 & 1);
    if (dvalid) { *(LAS f32x4*)(L + L_RED + dpart * 512 + d4 * 16) = qa; *(LAS f32x4*)(L + L_RED + dpart * 512 + 256 + d4 * 16) = fa; }
    BARRIER();
    ISSUE(DA, NQ, paced);
    BARRIER();
    v4u qf[3];
#pragma unroll
    for (int t = 0; t < 3; ++t) qf[t] = *(LAS v4u*)(L + L_Q + t * 128 + c8 * 16);
    const v2u tg = *(LAS v2u*)(L + L_TAG);
    const unsigned tlo = tg.x, thi = tg.y;
    float sc[8];
#pragma unroll
    for (int i = 0; i < 8; ++i) sc[i] = 0.f;
    sfor<3>([&](auto T) INL {
      constexpr int t = decltype(T)::value;
#pragma unroll
      for (int e = 0; e < 4; ++e) {
#pragma unroll
        for (int i = 0; i < 8; ++i) sc[i] = dot2(kd[3 * i + t][e], qf[t][e], sc[i]);
        PIN();
      }
      if constexpr (t > 0) paced(IC(NQ + t - 1));
    });
    sfor<2>([&](auto T) INL {
      constexpr int t = decltype(T)::value;
#pragma unroll
      for (int i = 4 * t; i < 4 * t + 4; ++i) { sc[i] = rbf(rbf(sum8(sc[i])) * 0.07216878364870322f); asm volatile("" : "+v"(sc[i])); }
      PIN();
      paced(IC(NQ + 2 + t));
    });
    float mx = -3.0e38f;
#pragma unroll
    for (int i = 0; i < 8; ++i) mx = fmaxf(mx, sc[i]);
    mx = fmaxf(mx, dpp<ROR8>(mx)); mx = max_x32(max_x16(mx));
    asm volatile("" : "+v"(mx));
    PIN();
    paced(IC(NQ + 4));
    v4u pop[8];
    float lsum = 0.f;
    sfor<2>([&](auto T) INL {
      constexpr int t = decltype(T)::value;
#pragma unroll
      for (int i = 4 * t; i < 4 * t + 4; ++i) {
        const float e = __builtin_amdgcn_exp2f((sc[i] - mx) * LOG2E);
        lsum += e;
        const unsigned pp = pk(e, e);
        pop[i] = v4u{pp & mask.x, pp & mask.y, pp & mask.z, pp & mask.w};
        asm volatile("" : "+v"(pop[i]));
      }
      PIN();
      paced(IC(NQ + 5 + t));
    });
    lsum = sum_groups(lsum);
    if (lane == 0) *(LAS f32x2*)(L + L_M + w * 8) = f32x2{mx, lsum};
    PIN();
    f32x4 oa[3];
#pragma unroll
    for (int t = 0; t < 3; ++t) oa[t] = f32x4{0.f, 0.f, 0.f, 0.f};
    int la = dvalid ? L_RED + dpart * 1024 + d4 * 16 : L_DUMP + lane * 16;
    asm volatile("" : "+v"(la));
    sfor<24>([&](auto J) INL {
      constexpr int j = decltype(J)::value;
      oa[j % 3] = __builtin_amdgcn_mfma_f32_16x16x32_bf16(__builtin_bit_cast(bf16x8, pop[j / 3]), __builtin_bit_cast(bf16x8, vd[j]), oa[j % 3], 0, 0, 0);
      PIN();
      if constexpr (24 + j + SW >= NQ + 7 && 24 + j + SW < 48 + CTOT) paced(IC(24 + j + SW));
      if constexpr (j == 23 - EARLY) BARRIER();
    });
#pragma unroll
    for (int t = 0; t < 3; ++t) *(LAS f32x4*)(L + la + t * 256) = oa[t];
    BARRIER();
    {
      const v2u hh = *(LAS v2u*)(L + L_H + (16 * w + 2 * g8) * 4);
      const f32x4 ba = gemv(hh.y, mask, wc[1], gemv(hh.x, mask, wc[0], f32x4{0.f, 0.f, 0.f, 0.f}));
      if (dvalid) *(LAS f32x4*)(L + L_RB + dpart * 256 + d4 * 16) = ba;
    }
    BARRIER();
    ISSUE(48 + CTOT, 62, issue);
    const rsrc_t rws = mk(p.ws, WS_END);
    int ooff = WS_O + (h * 8 + g8) * 1536 + (24 * w + c8) * 16, ls = c16 < 10 && hi == 0 ? L_RS + w * 320 + (2 * c16 + g16) * 16 : L_DUMP + lane * 16;
    int s_off = (WS_S + (20 * s + (lane >> 1)) * 1024 + h * 32 + (lane & 1) * 16) | (lane < 40 ? 0 : OOB), lr = L_RS + (lane < 40 ? lane : 0) * 8;
    asm volatile("" : "+v"(ooff), "+v"(ls), "+v"(s_off), "+v"(lr));
    v4u og[3];
    BARRIER();
#pragma unroll
    for (int j = 0; j < 3; ++j) og[j] = LD4(rws, ooff + 128 * j, 0, SC1);
    BARRIER();
    float f = *(LAS float*)(L + L_F + g8 * 4);
    asm volatile("" : "+v"(f));
    PIN();
    unsigned bad = STALE(og[0]) | STALE(og[1]) | STALE(og[2]);
    asm volatile("" : "+v"(bad));
    if (__builtin_expect(__any(bad != 0), 0)) {
#pragma nounroll
      for (unsigned spins = 0; spins < SPIN_CAP; ++spins) {
        asm volatile("buffer_load_dwordx4 %0, %3, %4, 0 offen sc1\n\tbuffer_load_dwordx4 %1, %3, %4, 0 offen offset:128 sc1\n\t"
                     "buffer_load_dwordx4 %2, %3, %4, 0 offen offset:256 sc1\n\ts_waitcnt vmcnt(0)"
                     : "+v"(og[0]), "+v"(og[1]), "+v"(og[2]) : "v"(ooff), "s"(rws) : "memory");
        if (!__any((STALE(og[0]) | STALE(og[1]) | STALE(og[2])) != 0)) break;
      }
    }
    {
      float y[3], z[3];
      int x[3];
#pragma unroll
      for (int j = 0; j < 3; ++j) { y[j] = f * asf(og[j].y); z[j] = f * asf(og[j].w); }
      PIN();
#pragma unroll
      for (int j = 0; j < 3; ++j) y[j] = fold32(y[j], z[j]);
      PIN();
#pragma unroll
      for (int j = 0; j < 3; ++j) y[j] += dpp<ROR8>(y[j]);
      PIN();
#pragma unroll
      for (int j = 0; j < 3; ++j) z[j] = dpp<ROR4>(y[j]);
      PIN();
#pragma unroll
      for (int j = 0; j < 3; ++j) { const auto t = __builtin_amdgcn_permlane16_swap(asu(y[j]), asu(z[j]), false, false); y[j] = asf(t[0]); z[j] = asf(t[1]); }
      PIN();
#pragma unroll
      for (int j = 0; j < 3; ++j) y[j] += z[j];
      PIN();
#pragma unroll
      for (int j = 0; j < 3; ++j) x[j] = (int)pk(y[j], y[j]);
      PIN();
      f32x4 sa = {0.f, 0.f, 0.f, 0.f};
      v4u op[13];
      auto build = [&](auto N) INL {
        constexpr int n = decltype(N)::value;
        const unsigned pp = (unsigned)__builtin_amdgcn_mov_dpp(x[n / 4], BCAST + n % 4, 0xf, 0xf, true);
        op[n] = v4u{pp & mask.x, pp & mask.y, pp & mask.z, pp & mask.w};
        PIN();
      };
      build(IC(0));
      sfor<12>([&](auto N) INL {
        constexpr int n = decltype(N)::value;
        if constexpr (n < 11) build(IC(n + 1));
        sa = __builtin_amdgcn_mfma_f32_16x16x32_bf16(__builtin_bit_cast(bf16x8, op[n]), __builtin_bit_cast(bf16x8, wc[2 + n]), sa, 0, 0, 0);
        PIN();
      });
      *(LAS f32x4*)(L + ls) = sa;
#pragma unroll
      for (int n = 2; n < 14; ++n) asm volatile("" :: "v"(wc[n]));
    }
    BARRIER();
    if (w == 1) {
      const f32x2 a = (*(LAS f32x2*)(L + lr) + *(LAS f32x2*)(L + lr + 320)) + (*(LAS f32x2*)(L + lr + 640) + *(LAS f32x2*)(L + lr + 960));
      PUT(a.x, a.y, s_off);
    }
  }
}
}

extern "C" void kernel_launch(void* const* d_in, const int* in_sizes, int n_in, void* d_out, int out_size, void* d_ws, size_t ws_size,
                              hipStream_t stream) {
  Params p;
  p.x = (const unsigned short*)d_in[0];
  p.k = (const unsigned char*)d_in[1];
  p.v = (const unsigned char*)d_in[2];
  p.wq = (const unsigned char*)d_in[3];
  p.wo = (const unsigned char*)d_in[4];
  p.wg = (const unsigned char*)d_in[6];
  p.wu = (const unsigned char*)d_in[7];
  p.wd = (const unsigned char*)d_in[8];
  p.out = (unsigned short*)d_out;
  p.ws = (unsigned char*)d_ws;
  hipLaunchKernelGGL(fused_decode, dim3(256), dim3(320), 0, stream, p.wq, p.wg, p.wu, p.x, p.ws, p.k, p.v, p.wo, p.wd, p.out);
}
```

```cpp
#include <hip/hip_runtime.h>
#include <hip/hip_bf16.h>
#include <utility>

namespace {
typedef unsigned v4u __attribute__((ext_vector_type(4)));
typedef unsigned v2u __attribute__((ext_vector_type(2)));
typedef unsigned v16u __attribute__((ext_vector_type(16)));
typedef float f32x4 __attribute__((ext_vector_type(4)));
typedef float f32x2 __attribute__((ext_vector_type(2)));
typedef __bf16 bf16x8 __attribute__((ext_vector_type(8)));
typedef __bf16 bf16x2 __attribute__((ext_vector_type(2)));
typedef unsigned long long u64;
typedef __amdgpu_buffer_rsrc_t rsrc_t;
#define LAS __attribute__((address_space(3)))

constexpr int D = 640, H = 32, DH = 192, SK = 8192, WIN = 2048, F = 1536;
constexpr int NT = 2, SC1 = 16;
constexpr int OOB = 0x40000000;
constexpr unsigned SPIN_CAP = 1u << 16;
constexpr float LOG2E = 1.4426950408889634f;
constexpr int CODE_BYTES = 10752;

constexpr int TAU0 = 150, TAU1 = 138, TAU2 = 156;
constexpr int NT1 = 21, NT2 = 36;
constexpr int TAUC = 85;
constexpr int NAB = 6, TA0 = 400, TA = 125;
constexpr int TOFF = 1200;
constexpr int STAG = 11;
constexpr int TFIN = 1750;
constexpr int SW = 13;
constexpr int NA0 = 3, DA = 6;
constexpr int NQ = 30;
constexpr int CTOT = 9;
constexpr int EARLY = 3;
static_assert(NQ >= 24 && NA0 <= DA && DA <= 11 && CTOT < SW && NQ + 7 >= 24 + SW, "every load of the stream is issued exactly once, before its use");
constexpr int due_ns(int n) { return n == 0 ? TOFF : due_ns(n - 1) + (n <= NT1 ? TAU0 : n <= NT2 ? TAU1 : n <= 50 ? TAU2 : TAUC) + (n == 48 ? 40 : 0); }

constexpr int WS_EPOCH = 0;
constexpr int WS_Q = 4096;
constexpr int WS_G = WS_Q + 32 * 5120;
constexpr int WS_O = WS_G + 24 * 5120;
constexpr int WS_M = WS_O + 256 * 1536;
constexpr int WS_B = WS_M + 256 * 128;
constexpr int WS_S = WS_B + 24 * 5120;
constexpr int WS_HQ = WS_S + 160 * 1024;
constexpr int WS_HO = WS_HQ + 32 * 128;
constexpr int WS_END = WS_HO + 32 * 128;

constexpr int L_RED = 0;
constexpr int L_X = 8192;
constexpr int L_Q = L_X + 2560;
constexpr int L_H = L_Q + 384;
constexpr int L_M = L_H + 256;
constexpr int L_TAG = L_M + 32;
constexpr int L_F = L_M + 64;
constexpr int L_RB = L_F + 32;
constexpr int L_RS = L_RB + 2048;
constexpr int L_DUMP = L_RS + 1280;
constexpr int LDS_BYTES = 88 * 1024;

struct Params {
  const unsigned char* wq; const unsigned char* wg; const unsigned char* wu; const unsigned char* k; const unsigned char* v;
  const unsigned char* wo; const unsigned char* wd; const unsigned short* x; unsigned short* out; unsigned char* ws;
};

__device__ __forceinline__ rsrc_t mk(const void* p, int bytes) { return __builtin_amdgcn_make_buffer_rsrc((void*)p, 0, bytes, 0x00020000); }
__device__ __forceinline__ unsigned pk(float lo, float hi) { bf16x2 v = {(__bf16)lo, (__bf16)hi}; return __builtin_bit_cast(unsigned, v); }
__device__ __forceinline__ float lo16(unsigned a) { return __builtin_bit_cast(float, a << 16); }
__device__ __forceinline__ float hi16(unsigned a) { return __builtin_bit_cast(float, a & 0xffff0000u); }
__device__ __forceinline__ float rbf(float a) { return lo16(pk(a, a)); }
__device__ __forceinline__ float asf(unsigned a) { return __builtin_bit_cast(float, a); }
__device__ __forceinline__ unsigned asu(float a) { return __builtin_bit_cast(unsigned, a); }
template <int CTRL> __device__ __forceinline__ float dpp(float x) {
  return __builtin_bit_cast(float, __builtin_amdgcn_mov_dpp(__builtin_bit_cast(int, x), CTRL, 0xf, 0xf, true));
}
constexpr int XOR1 = 0xB1, XOR2 = 0x4E, XOR7 = 0x141, ROR8 = 0x128, ROR4 = 0x124, BCAST = 0x150;
__device__ __forceinline__ float sum8(float x) { x += dpp<XOR1>(x); x += dpp<XOR2>(x); x += dpp<XOR7>(x); return x; }
__device__ __forceinline__ float max8(float x) { x = fmaxf(x, dpp<XOR1>(x)); x = fmaxf(x, dpp<XOR2>(x)); x = fmaxf(x, dpp<XOR7>(x)); return x; }
__device__ __forceinline__ float sum_x16(float x) { auto s = __builtin_amdgcn_permlane16_swap(asu(x), asu(x), false, false); return asf(s[0]) + asf(s[1]); }
__device__ __forceinline__ float sum_x32(float x) { auto s = __builtin_amdgcn_permlane32_swap(asu(x), asu(x), false, false); return asf(s[0]) + asf(s[1]); }
__device__ __forceinline__ float fold32(float a, float b) { auto s = __builtin_amdgcn_permlane32_swap(asu(a), asu(b), false, false); return asf(s[0]) + asf(s[1]); }
__device__ __forceinline__ float max_x16(float x) { auto s = __builtin_amdgcn_permlane16_swap(asu(x), asu(x), false, false); return fmaxf(asf(s[0]), asf(s[1])); }
__device__ __forceinline__ float max_x32(float x) { auto s = __builtin_amdgcn_permlane32_swap(asu(x), asu(x), false, false); return fmaxf(asf(s[0]), asf(s[1])); }
__device__ __forceinline__ float sum_groups(float x) { x += dpp<ROR8>(x); return sum_x32(sum_x16(x)); }
__device__ __forceinline__ float dot2(unsigned a, unsigned b, float c) {
  return __builtin_amdgcn_fdot2_f32_bf16(__builtin_bit_cast(bf16x2, a), __builtin_bit_cast(bf16x2, b), c, false);
}
__device__ __forceinline__ f32x4 gemv(unsigned pp, v4u mask, v4u b, f32x4 c) {
  const v4u a = {pp & mask.x, pp & mask.y, pp & mask.z, pp & mask.w};
  return __builtin_amdgcn_mfma_f32_16x16x32_bf16(__builtin_bit_cast(bf16x8, a), __builtin_bit_cast(bf16x8, b), c, 0, 0, 0);
}
template <class Fn, int... I> __device__ __forceinline__ void sfor_(Fn&& f, std::integer_sequence<int, I...>) { (f(std::integral_constant<int, I>{}), ...); }
template <int N, class Fn> __device__ __forceinline__ void sfor(Fn&& f) { sfor_(f, std::make_integer_sequence<int, N>{}); }
#define INL __attribute__((always_inline))
#define IC(n) std::integral_constant<int, (n)>{}
#define RELOAD() asm volatile("" ::: "memory")
#define LD4(rs, vo, so, aux) __builtin_amdgcn_raw_buffer_load_b128(rs, vo, so, aux)
#define PUT(a, b, vo) __builtin_amdgcn_raw_buffer_store_b128(v4u{tlo, asu(a), thi, asu(b)}, rws, vo, 0, SC1)
#define HINT(off) __builtin_amdgcn_raw_buffer_store_b32(tlo, rws, lane == 0 ? (off) : OOB, 0, SC1)
#define STALE(g) (((g).x ^ tlo) | ((g).z ^ thi))
#define CONFIRM(g, off, live) do { if (__builtin_expect(__any((live) && STALE(g) != 0), 0)) { _Pragma("nounroll") \
    for (unsigned spins = 0; spins < SPIN_CAP; ++spins) { \
      asm volatile("buffer_load_dwordx4 %0, %1, %2, 0 offen sc1\n\ts_waitcnt vmcnt(0)" : "+v"(g) : "v"(off), "s"(rws) : "memory"); \
      if (!__any((live) && STALE(g) != 0)) break; } } } while (0)
#define WAIT_UNTIL(t0, ns) asm volatile("1:\n\ts_memrealtime vcc\n\ts_waitcnt lgkmcnt(0)\n\ts_sub_u32 vcc_lo, vcc_lo, %0\n\t" \
    "s_cmpk_lt_u32 vcc_lo, %1\n\ts_cbranch_scc1 1b" :: "s"(t0), "n"((ns) / 10) : "vcc", "scc", "memory")
#define PIN() __builtin_amdgcn_sched_barrier(0)
#define BARRIER() do { PIN(); asm volatile("s_waitcnt lgkmcnt(0)\n\ts_barrier" ::: "memory"); PIN(); } while (0)

template <int N> __device__ __forceinline__ void wait_hint(const unsigned char* ip, unsigned t) {
  typedef u64 v8l __attribute__((ext_vector_type(8)));
  const u64 tt = ((u64)t << 32) | t;
#pragma nounroll
  for (int spins = 0; spins < 256; ++spins) {
    v8l v;
    asm volatile("s_load_dwordx16 %0, %1, 0x0 glc\n\ts_waitcnt lgkmcnt(0)" : "=&s"(v) : "s"(ip) : "memory");
    u64 bad = 0;
#pragma unroll
    for (int i = 0; i < N / 2; ++i) bad |= v[i] ^ tt;
    asm volatile("" : "+s"(bad), "+s"(spins));
    if (bad == 0) break;
  }
}

__global__ void __launch_bounds__(320, 2) __attribute__((aligned(4096)))
fused_decode(const unsigned char* a_wq, const unsigned char* a_wg, const unsigned char* a_wu, const unsigned short* a_x, unsigned char* a_ws,
             const unsigned char* a_k, const unsigned char* a_v, const unsigned char* a_wo, const unsigned char* a_wd, unsigned short* a_out) {
  __shared__ __attribute__((aligned(16))) unsigned char smem[LDS_BYTES];
  LAS unsigned char* L = (LAS unsigned char*)smem;
  v16u ka; v4u kb;
  asm volatile("s_load_dwordx16 %0, %2, 0x0\n\ts_load_dwordx4 %1, %2, 0x40" : "=&s"(ka), "=&s"(kb) : "s"(__builtin_amdgcn_kernarg_segment_ptr()));
  const int b = blockIdx.x, tid = threadIdx.x, lane = tid & 63, w = __builtin_amdgcn_readfirstlane(tid >> 6);
  unsigned pf0, pf1;
  {
    const rsrc_t rc = mk((const void*)(__builtin_amdgcn_s_getpc() & ~127ull), CODE_BYTES);
    const int po = (tid ^ 256) * 128;
    pf0 = __builtin_amdgcn_raw_buffer_load_b32(rc, po, 0, 0);
    pf1 = __builtin_amdgcn_raw_buffer_load_b32(rc, po + 8192, 0, 0);
  }
  if (w == 4) __builtin_amdgcn_s_setprio(3);
  PIN();
  const int idx = b >> 3, h = 4 * (b & 7) + (idx & 3), s = idx >> 2;
  const int f3 = (idx * 26) >> 8, cf = 3 * (b & 7) + f3, fj = idx - 10 * f3, fo = idx < 30 ? 0 : OOB;
  const int g8 = lane >> 3, c8 = lane & 7, g16 = lane >> 4, c16 = lane & 15, hi = lane >> 5, l32 = lane & 31;
  const int dpart = 2 * w + (g8 & 1);
  const int R = 240 * s - (s & 4) * 4 + 32 * dpart + 8 * g16, cb = (R >= 640) + (R >= 1280), rq0 = R - 640 * cb;
  const int rf0 = 128 * (fj - 5 * (fj >= 5)) + 32 * w + 4 * g8;
  const bool rok = (unsigned)(R - 240 * s) < 240u;
  int vq = (rq0 * 12288 + h * 384 + cb * 128 + c8 * 16) | (rok ? 0 : OOB), vf = (rf0 * 3072 + cf * 128 + c8 * 16) | fo;
  int vkv = (h * SK + (SK - WIN) + 32 * s + 8 * w + g8) * 384 + c8 * 16;
  int vx = lane * 20;
  v4u xa;
  v2u ep;
  unsigned xb, t_own;
  int role = w;
  asm volatile("" : "+v"(vq), "+v"(vf), "+v"(vkv), "+v"(vx), "=v"(xa), "=v"(xb), "=v"(ep));
  asm volatile("" : "=s"(t_own), "+s"(role));
  PIN();
  asm volatile("s_waitcnt lgkmcnt(0)" : "+s"(ka), "+s"(kb) :: "memory");
#define ARG(v, i) (unsigned char*)(((u64)(v)[2 * (i) + 1] << 32) | (v)[2 * (i)])
  const Params p = {ARG(ka, 0), ARG(ka, 1), ARG(ka, 2), ARG(ka, 5), ARG(ka, 6), ARG(ka, 7), ARG(kb, 0), (const unsigned short*)ARG(ka, 3), (unsigned short*)ARG(kb, 1), ARG(ka, 4)};
  if (w == 4) {
    const rsrc_t rx = mk(p.x, D * 2);
    xa = LD4(rx, vx, 0, 0);
    xb = __builtin_amdgcn_raw_buffer_load_b32(rx, vx + 16, 0, 0);
    ep = __builtin_amdgcn_raw_buffer_load_b64(mk(p.ws, WS_END), WS_EPOCH, 0, SC1);
    u64 t;
    asm volatile("s_memrealtime %0\n\ts_waitcnt lgkmcnt(0)" : "=s"(t) :: "memory");
    t_own = (unsigned)t;
  }

  if (role == 4) {
    const rsrc_t rws = mk(p.ws, WS_END), rx = mk(p.x, D * 2);
    const int rj = 16 * ((b & 7) + (idx >> 4) * 8) + (idx & 15);
    const unsigned short xo = __builtin_amdgcn_raw_buffer_load_b16(rx, (4 * rj + 2 * (lane & 1) + hi) * 2, 0, 0);
    {
      float xv[10] = {lo16(xa.x), hi16(xa.x), lo16(xa.y), hi16(xa.y), lo16(xa.z), hi16(xa.z), lo16(xa.w), hi16(xa.w), lo16(xb), hi16(xb)};
      float sm = 0.f;
#pragma unroll
      for (int i = 0; i < 10; ++i) sm += xv[i];
      const float mean = sum_groups(sum8(sm)) * (1.f / D);
      float sv = 0.f;
#pragma unroll
      for (int i = 0; i < 10; ++i) { const float d = xv[i] - mean; sv = fmaf(d, d, sv); }
      const float var = sum_groups(sum8(sv)) * (1.f / D);
      const float mb = rbf(mean), sd = rbf(__builtin_amdgcn_sqrtf(rbf(rbf(var) + 1e-5f))), rs = __builtin_amdgcn_rcpf(sd);
      unsigned xp[10];
#pragma unroll
      for (int i = 0; i < 10; ++i) { const float d = rbf(xv[i] - mb), q = d * rs, r = fmaf(fmaf(-q, sd, d), rs, q); xp[i] = pk(r, r); }
#pragma unroll
      for (int i = 0; i < 5; ++i) *(LAS v2u*)(L + L_X + lane * 40 + i * 8) = v2u{xp[2 * i], xp[2 * i + 1]};
    }
    BARRIER();
    const u64 tag = (((u64)__builtin_amdgcn_readfirstlane(ep.y) << 32) | (unsigned)__builtin_amdgcn_readfirstlane(ep.x)) + 0x9E3779B97F4A7C15ull;
    const unsigned tlo = (unsigned)tag, thi = (unsigned)(tag >> 32);
    *(LAS v2u*)(L + L_TAG) = v2u{tlo, thi};
    if (lane < 8) *(LAS float*)(L + L_M + lane * 4) = -1.f;
    asm volatile("" :: "v"(pf0), "v"(pf1));
    const int nd = s == 2 ? 5 : s == 5 ? 3 : 8;
    int q_off = (WS_Q + h * 5120 + (s + (3 * s >> 3)) * 512 + lane * 16) | (hi == 0 || nd < 8 ? 0 : OOB);
    int g_off = (WS_G + cf * 5120 + fj * 512 + lane * 16) | (hi == 0 ? fo : OOB);
    int hq_off = lane == 0 ? WS_HQ + h * 128 + s * 4 : OOB;
    asm volatile("" : "+v"(q_off), "+v"(g_off), "+v"(hq_off));
    BARRIER();
    {
      f32x2 t[8], c[8];
#pragma unroll
      for (int i = 0; i < 8; ++i) { t[i] = *(LAS f32x2*)(L + L_RED + i * 512 + l32 * 8); c[i] = *(LAS f32x2*)(L + L_RED + i * 512 + 256 + l32 * 8); }
      f32x2 s1 = t[0] + t[1] + t[2], s2 = t[3] + t[4], s3 = t[5] + t[6] + t[7];
      asm volatile("" : "+v"(s1), "+v"(s2), "+v"(s3));
      const f32x2 z = {0.f, 0.f};
      const f32x2 a = (hi ? z : s1) + ((nd == 3) == (hi != 0) ? s2 : z) + ((nd == 8) == (hi == 0) ? s3 : z);
      PUT(a.x, a.y, q_off);
      __builtin_amdgcn_raw_buffer_store_b32(tlo, rws, hq_off, 0, SC1);
      const f32x2 g = ((c[0] + c[1]) + (c[2] + c[3])) + ((c[4] + c[5]) + (c[6] + c[7]));
      PUT(g.x, g.y, g_off);
    }
    {
      v4u r[5];
      wait_hint<8>(p.ws + WS_HQ + h * 128, tlo);
#pragma nounroll
      for (unsigned spins = 0; spins < SPIN_CAP; ++spins) {
        RELOAD();
#pragma unroll
        for (int i = 0; i < 5; ++i) r[i] = LD4(rws, WS_Q + h * 5120 + lane * 16, i * 1024, SC1);
        if (!__any((STALE(r[0]) | STALE(r[1]) | STALE(r[2]) | STALE(r[3]) | STALE(r[4])) != 0)) break;
      }
      const float a0 = asf(r[0].y) + (hi ? 0.f : asf(r[1].y)), a1 = asf(r[0].w) + (hi ? 0.f : asf(r[1].w));
      const float b0 = asf(r[2].y) + asf(hi ? r[1].y : r[3].y), b1 = asf(r[2].w) + asf(hi ? r[1].w : r[3].w);
      const float c0 = asf(r[4].y) + (hi ? asf(r[3].y) : 0.f), c1 = asf(r[4].w) + (hi ? asf(r[3].w) : 0.f);
      *(LAS unsigned*)(L + L_Q + lane * 4) = pk(fold32(a0, b0), fold32(a1, b1));
      *(LAS unsigned*)(L + L_Q + 256 + l32 * 4) = pk(sum_x32(c0), sum_x32(c1));
    }
    BARRIER();
    if (idx < 30) {
      v4u r[5];
#pragma nounroll
      for (unsigned spins = 0; spins < SPIN_CAP; ++spins) {
        RELOAD();
#pragma unroll
        for (int i = 0; i < 5; ++i) r[i] = LD4(rws, WS_G + cf * 5120 + lane * 16, i * 1024, SC1);
        if (!__any((STALE(r[0]) | STALE(r[1]) | STALE(r[2]) | STALE(r[3]) | STALE(r[4])) != 0)) break;
      }
      const float g0 = sum_x32(asf(r[0].y) + asf(r[1].y) + (hi ? 0.f : asf(r[2].y))), g1 = sum_x32(asf(r[0].w) + asf(r[1].w) + (hi ? 0.f : asf(r[2].w)));
      const float u0 = sum_x32(asf(r[3].y) + asf(r[4].y) + (hi ? asf(r[2].y) : 0.f)), u1 = sum_x32(asf(r[3].w) + asf(r[4].w) + (hi ? asf(r[2].w) : 0.f));
      const float h0 = rbf(g0) * rbf(u0), h1 = rbf(g1) * rbf(u1);
      *(LAS v2u*)(L + L_H + l32 * 8) = v2u{pk(h0, h0), pk(h1, h1)};
    } else {
      *(LAS v2u*)(L + L_H + l32 * 8) = v2u{0u, 0u};
    }
    float wt[4];
    {
      f32x4 a, c;
#pragma nounroll
      for (unsigned spins = 0; spins < (SPIN_CAP << 4); ++spins) {
        RELOAD();
        a = *(LAS f32x4*)(L + L_M); c = *(LAS f32x4*)(L + L_M + 16);
        if (!__any(a.y < 0.f || a.w < 0.f || c.y < 0.f || c.w < 0.f)) break;
        __builtin_amdgcn_s_sleep(2);
      }
      const float mx = fmaxf(fmaxf(a.x, a.z), fmaxf(c.x, c.z));
      wt[0] = __builtin_amdgcn_exp2f((a.x - mx) * LOG2E); wt[1] = __builtin_amdgcn_exp2f((a.z - mx) * LOG2E);
      wt[2] = __builtin_amdgcn_exp2f((c.x - mx) * LOG2E); wt[3] = __builtin_amdgcn_exp2f((c.z - mx) * LOG2E);
      PUT(mx, (wt[0] * a.y + wt[1] * a.w) + (wt[2] * c.y + wt[3] * c.w), lane == 0 ? WS_M + (h * 8 + s) * 128 : OOB);
    }
    int o_off = WS_O + (h * 8 + s) * 1536 + lane * 16, o_of1 = (o_off + 1024) | (hi ? OOB : 0);
    asm volatile("" : "+v"(o_off), "+v"(o_of1), "+v"(wt[0]), "+v"(wt[1]), "+v"(wt[2]), "+v"(wt[3]));
    BARRIER();
    HINT(WS_HO + h * 128 + s * 4);
    const int moff = (WS_M + (h * 8 + c8) * 128) | (lane < 8 ? 0 : OOB);
    v4u ml = LD4(rws, moff, 0, SC1);
    BARRIER();
    {
      f32x2 t[8], u[8];
#pragma unroll
      for (int i = 0; i < 8; ++i) t[i] = *(LAS f32x2*)(L + L_RED + i * 1024 + lane * 8);
#pragma unroll
      for (int i = 0; i < 8; ++i) u[i] = *(LAS f32x2*)(L + L_RED + i * 1024 + 512 + l32 * 8);
      PIN();
      const f32x2 a = (wt[0] * (t[0] + t[1]) + wt[1] * (t[2] + t[3])) + (wt[2] * (t[4] + t[5]) + wt[3] * (t[6] + t[7]));
      const f32x2 c = (wt[0] * (u[0] + u[1]) + wt[1] * (u[2] + u[3])) + (wt[2] * (u[4] + u[5]) + wt[3] * (u[6] + u[7]));
      PUT(a.x, a.y, o_off);
      PUT(c.x, c.y, o_of1);
    }
    BARRIER();
    wait_hint<8>(p.ws + WS_HO + h * 128, tlo);
    asm volatile("s_waitcnt vmcnt(0)" ::: "memory");
    BARRIER();
    {
      CONFIRM(ml, moff, lane < 8);
      const float M = max8(asf(ml.y));
      const float e = __builtin_amdgcn_exp2f((asf(ml.y) - M) * LOG2E);
      const float f = e * __builtin_amdgcn_rcpf(sum8(e * asf(ml.w)));
      if (lane < 8) *(LAS float*)(L + L_F + lane * 4) = f;
    }
    BARRIER();
    {
      f32x2 a[8];
#pragma unroll
      for (int i = 0; i < 8; ++i) a[i] = *(LAS f32x2*)(L + L_RB + i * 256 + l32 * 8);
      const f32x2 t = ((a[0] + a[1]) + (a[2] + a[3])) + ((a[4] + a[5]) + (a[6] + a[7]));
      PUT(t.x, t.y, (WS_B + cf * 5120 + fj * 512 + lane * 16) | (hi == 0 ? fo : OOB));
    }
    const int boff = (WS_B + (lane >> 1) * 5120 + rj * 32 + (lane & 1) * 16) | (lane < 48 ? 0 : OOB);
    BARRIER();
    if (rj < 160) {
      v4u r = LD4(rws, boff, 0, SC1);
      CONFIRM(r, boff, lane < 48);
      float t = fold32(asf(r.y), asf(r.w));
      t += dpp<XOR2>(t); t += dpp<ROR4>(t); t += dpp<ROR8>(t); t = sum_x16(t);
      const float bv = rbf(t);
      float gate = rbf(bv / (1.f + __expf(-bv))), xv = lo16(xo);
      int oo = l32 < 2 ? (4 * rj + 2 * (lane & 1) + hi) * 2 : OOB;
      int r_off = WS_S + rj * 1024 + lane * 16;
      rsrc_t rout = mk(p.out, D * 2);
      asm volatile("" : "+v"(gate), "+v"(xv), "+v"(oo), "+v"(r_off));
      asm volatile("" : "+s"(rout));
      WAIT_UNTIL(t_own, due_ns(61) + TFIN);
      {
        unsigned left = SPIN_CAP;
        u64 tmp;
        asm volatile("1:\n\tbuffer_load_dwordx4 v[40:43], %3, %4, 0 offen sc1\n\ts_sub_u32 %1, %1, 1\n\ts_waitcnt vmcnt(0)\n\ts_cbranch_scc1 2f\n\t"
                     "v_cmp_ne_u32 vcc, %5, v40\n\tv_cmp_ne_u32 %2, %6, v42\n\ts_or_b64 vcc, vcc, %2\n\ts_cbranch_scc1 1b\n2:"
                     : "=&{v[40:43]}"(r), "+s"(left), "=&s"(tmp) : "v"(r_off), "s"(rws), "s"(tlo), "s"(thi) : "vcc", "scc", "memory");
      }
      t = fold32(asf(r.y), asf(r.w));
      t += dpp<XOR2>(t); t += dpp<ROR4>(t); t += dpp<ROR8>(t); t = sum_x16(t);
      const float res = xv + rbf(rbf(t) * gate);
      __builtin_amdgcn_raw_buffer_store_b16((unsigned short)pk(res, res), rout, oo, 0, 0);
      if (b == 0) __builtin_amdgcn_raw_buffer_store_b64(v2u{tlo, thi}, rws, lane == 0 ? WS_EPOCH : OOB, 0, SC1);
    }
  } else {
    v4u wa[12];
    u64 t_entry;
    asm volatile("s_memrealtime %0" : "=s"(t_entry) :: "memory");
    PIN();
    {
      const rsrc_t rq = mk(p.wq, D * H * DH * 2);
      sfor<12>([&](auto I) INL {
        constexpr int i = decltype(I)::value;
        if constexpr (i == NAB) asm volatile("s_waitcnt lgkmcnt(0)" : "+s"(t_entry) :: "memory");
        if constexpr (i >= NAB)
          WAIT_UNTIL((unsigned)t_entry, TA0 + (i - NAB) * TA);
        PIN();
        if constexpr (i < 8) wa[i] = LD4(rq, vq, i * 12288, NT); else wa[i] = LD4(mk(fj < 5 ? p.wg : p.wu, D * F * 2), vf, (i - 8) * 3072, NT);
        PIN();
      });
    }
    const rsrc_t rk = mk(p.k, H * SK * DH * 2), rv = mk(p.v, H * SK * DH * 2), ro = mk(p.wo, H * DH * D * 2), rd = mk(p.wd, F * D * 2);
    const int vo = ((192 * h + 48 * w + 8 * (g16 & 1) + (g16 >> 1)) * 1280 + (10 * s + c16) * 16) | (c16 < 10 ? 0 : OOB);
    const int vdn = ((64 * cf + 16 * w + 2 * g8) * 1280 + fj * 128 + c8 * 16) | fo;
    const unsigned t_start = (unsigned)t_entry, t_stag = t_start + (h & 3) * STAG;
    v4u kd[24], vd[24], wc[14];
    auto issue = [&](auto N) INL {
      constexpr int n = decltype(N)::value;
      if constexpr (n < 24) kd[n] = LD4(rk, vkv + (n % 3) * 128, ((n / 3 + h) & 7) * 98304, NT);
      else if constexpr (n < 48) vd[n - 24] = LD4(rv, vkv + (n % 3) * 128, ((n / 3 + h) & 7) * 98304, NT);
      else if constexpr (n < 50) wc[n - 48] = LD4(rd, vdn, (n - 48) * 1280, NT);
      else wc[n - 48] = LD4(ro, vo, (16 * ((n - 50) >> 2) + 2 * ((n - 50) & 3)) * 1280, NT);
      PIN();
    };
    auto paced = [&](auto N) INL {
      constexpr int n = decltype(N)::value;
      if constexpr (n >= 48 || n % 3 == 0) {
        asm volatile("s_waitcnt vmcnt(%0)" :: "n"(n < 48 ? SW - 3 : SW - 1) : "memory");
        WAIT_UNTIL(n < 48 ? t_stag : t_start, due_ns(n));
      }
      PIN();
      issue(N);
    };
#define ISSUE(lo, hi, fn) sfor<(hi) - (lo)>([&](auto I_) INL { fn(IC((lo) + decltype(I_)::value)); })
    v4u mask;
    {
      const unsigned hm = (lane & 1) ? 0xffff0000u : 0x0000ffffu;
      const int dsel = c8 >> 1;
      mask = v4u{dsel == 0 ? hm : 0u, dsel == 1 ? hm : 0u, dsel == 2 ? hm : 0u, dsel == 3 ? hm : 0u};
    }
    asm volatile("" :: "v"(pf0), "v"(pf1));
    ISSUE(0, NA0, paced);
    BARRIER();
    v4u xn[3];
    xn[0] = *(LAS v4u*)(L + L_X + (rok ? rq0 : 0) * 4); xn[1] = *(LAS v4u*)(L + L_X + (rok ? rq0 : 0) * 4 + 16); xn[2] = *(LAS v4u*)(L + L_X + rf0 * 4);
    f32x4 qa = {0.f, 0.f, 0.f, 0.f}, fa = {0.f, 0.f, 0.f, 0.f};
    sfor<12>([&](auto J) INL {
      constexpr int j = decltype(J)::value;
      if constexpr (j < 8) qa = gemv(xn[j / 4][j % 4], mask, wa[j], qa); else fa = gemv(xn[2][j % 4], mask, wa[j], fa);
      PIN();
      if constexpr (j >= 11 - DA + NA0 && j < 11) paced(IC(j - (11 - DA)));
    });
    const bool dvalid = hi == (g8 & 1);
    const int d4 = 2 * c8 + (g16 & 1);
    if (dvalid) { *(LAS f32x4*)(L + L_RED + dpart * 512 + d4 * 16) = qa; *(LAS f32x4*)(L + L_RED + dpart * 512 + 256 + d4 * 16) = fa; }
    BARRIER();
    ISSUE(DA, NQ, paced);
    BARRIER();
    v4u qf[3];
#pragma unroll
    for (int t = 0; t < 3; ++t) qf[t] = *(LAS v4u*)(L + L_Q + t * 128 + c8 * 16);
    const v2u tg = *(LAS v2u*)(L + L_TAG);
    const unsigned tlo = tg.x, thi = tg.y;
    float sc[8];
#pragma unroll
    for (int i = 0; i < 8; ++i) sc[i] = 0.f;
    sfor<3>([&](auto T) INL {
      constexpr int t = decltype(T)::value;
#pragma unroll
      for (int e = 0; e < 4; ++e) {
#pragma unroll
        for (int i = 0; i < 8; ++i) sc[i] = dot2(kd[3 * i + t][e], qf[t][e], sc[i]);
        PIN();
      }
      if constexpr (t > 0) paced(IC(NQ + t - 1));
    });
    sfor<2>([&](auto T) INL {
      constexpr int t = decltype(T)::value;
#pragma unroll
      for (int i = 4 * t; i < 4 * t + 4; ++i) { sc[i] = rbf(rbf(sum8(sc[i])) * 0.07216878364870322f); asm volatile("" : "+v"(sc[i])); }
      PIN();
      paced(IC(NQ + 2 + t));
    });
    float mx = -3.0e38f;
#pragma unroll
    for (int i = 0; i < 8; ++i) mx = fmaxf(mx, sc[i]);
    mx = fmaxf(mx, dpp<ROR8>(mx)); mx = max_x32(max_x16(mx));
    asm volatile("" : "+v"(mx));
    PIN();
    paced(IC(NQ + 4));
    v4u pop[8];
    float lsum = 0.f;
    sfor<2>([&](auto T) INL {
      constexpr int t = decltype(T)::value;
#pragma unroll
      for (int i = 4 * t; i < 4 * t + 4; ++i) {
        const float e = __builtin_amdgcn_exp2f((sc[i] - mx) * LOG2E);
        lsum += e;
        const unsigned pp = pk(e, e);
        pop[i] = v4u{pp & mask.x, pp & mask.y, pp & mask.z, pp & mask.w};
        asm volatile("" : "+v"(pop[i]));
      }
      PIN();
      paced(IC(NQ + 5 + t));
    });
    lsum = sum_groups(lsum);
    if (lane == 0) *(LAS f32x2*)(L + L_M + w * 8) = f32x2{mx, lsum};
    PIN();
    f32x4 oa[3];
#pragma unroll
    for (int t = 0; t < 3; ++t) oa[t] = f32x4{0.f, 0.f, 0.f, 0.f};
    int la = dvalid ? L_RED + dpart * 1024 + d4 * 16 : L_DUMP + lane * 16;
    asm volatile("" : "+v"(la));
    sfor<24>([&](auto J) INL {
      constexpr int j = decltype(J)::value;
      oa[j % 3] = __builtin_amdgcn_mfma_f32_16x16x32_bf16(__builtin_bit_cast(bf16x8, pop[j / 3]), __builtin_bit_cast(bf16x8, vd[j]), oa[j % 3], 0, 0, 0);
      PIN();
      if constexpr (24 + j + SW >= NQ + 7 && 24 + j + SW < 48 + CTOT) paced(IC(24 + j + SW));
      if constexpr (j == 23 - EARLY) BARRIER();
    });
#pragma unroll
    for (int t = 0; t < 3; ++t) *(LAS f32x4*)(L + la + t * 256) = oa[t];
    BARRIER();
    {
      const v2u hh = *(LAS v2u*)(L + L_H + (16 * w + 2 * g8) * 4);
      const f32x4 ba = gemv(hh.y, mask, wc[1], gemv(hh.x, mask, wc[0], f32x4{0.f, 0.f, 0.f, 0.f}));
      if (dvalid) *(LAS f32x4*)(L + L_RB + dpart * 256 + d4 * 16) = ba;
    }
    BARRIER();
    ISSUE(48 + CTOT, 62, issue);
    const rsrc_t rws = mk(p.ws, WS_END);
    int ooff = WS_O + (h * 8 + g8) * 1536 + (24 * w + c8) * 16, ls = c16 < 10 && hi == 0 ? L_RS + w * 320 + (2 * c16 + g16) * 16 : L_DUMP + lane * 16;
    int s_off = (WS_S + (20 * s + (lane >> 1)) * 1024 + h * 32 + (lane & 1) * 16) | (lane < 40 ? 0 : OOB), lr = L_RS + (lane < 40 ? lane : 0) * 8;
    asm volatile("" : "+v"(ooff), "+v"(ls), "+v"(s_off), "+v"(lr));
    v4u og[3];
    BARRIER();
#pragma unroll
    for (int j = 0; j < 3; ++j) og[j] = LD4(rws, ooff + 128 * j, 0, SC1);
    BARRIER();
    float f = *(LAS float*)(L + L_F + g8 * 4);
    asm volatile("" : "+v"(f));
    PIN();
    unsigned bad = STALE(og[0]) | STALE(og[1]) | STALE(og[2]);
    asm volatile("" : "+v"(bad));
    if (__builtin_expect(__any(bad != 0), 0)) {
#pragma nounroll
      for (unsigned spins = 0; spins < SPIN_CAP; ++spins) {
        asm volatile("buffer_load_dwordx4 %0, %3, %4, 0 offen sc1\n\tbuffer_load_dwordx4 %1, %3, %4, 0 offen offset:128 sc1\n\t"
                     "buffer_load_dwordx4 %2, %3, %4, 0 offen offset:256 sc1\n\ts_waitcnt vmcnt(0)"
                     : "+v"(og[0]), "+v"(og[1]), "+v"(og[2]) : "v"(ooff), "s"(rws) : "memory");
        if (!__any((STALE(og[0]) | STALE(og[1]) | STALE(og[2])) != 0)) break;
      }
    }
    {
      float y[3], z[3];
      int x[3];
#pragma unroll
      for (int j = 0; j < 3; ++j) { y[j] = f * asf(og[j].y); z[j] = f * asf(og[j].w); }
      PIN();
#pragma unroll
      for (int j = 0; j < 3; ++j) y[j] = fold32(y[j], z[j]);
      PIN();
#pragma unroll
      for (int j = 0; j < 3; ++j) y[j] += dpp<ROR8>(y[j]);
      PIN();
#pragma unroll
      for (int j = 0; j < 3; ++j) z[j] = dpp<ROR4>(y[j]);
      PIN();
#pragma unroll
      for (int j = 0; j < 3; ++j) { const auto t = __builtin_amdgcn_permlane16_swap(asu(y[j]), asu(z[j]), false, false); y[j] = asf(t[0]); z[j] = asf(t[1]); }
      PIN();
#pragma unroll
      for (int j = 0; j < 3; ++j) y[j] += z[j];
      PIN();
#pragma unroll
      for (int j = 0; j < 3; ++j) x[j] = (int)pk(y[j], y[j]);
      PIN();
      f32x4 sa = {0.f, 0.f, 0.f, 0.f};
      v4u op[12];
      auto build = [&](auto N) INL {
        constexpr int n = decltype(N)::value;
        const unsigned pp = (unsigned)__builtin_amdgcn_mov_dpp(x[n / 4], BCAST + n % 4, 0xf, 0xf, true);
        op[n] = v4u{pp & mask.x, pp & mask.y, pp & mask.z, pp & mask.w};
        PIN();
      };
      build(IC(0));
      sfor<12>([&](auto N) INL {
        constexpr int n = decltype(N)::value;
        if constexpr (n < 11) build(IC(n + 1));
        sa = __builtin_amdgcn_mfma_f32_16x16x32_bf16(__builtin_bit_cast(bf16x8, op[n]), __builtin_bit_cast(bf16x8, wc[2 + n]), sa, 0, 0, 0);
        PIN();
      });
      *(LAS f32x4*)(L + ls) = sa;
#pragma unroll
      for (int n = 2; n < 14; ++n) asm volatile("" :: "v"(wc[n]));
    }
    BARRIER();
    if (w == 1) {
      const f32x2 a = (*(LAS f32x2*)(L + lr) + *(LAS f32x2*)(L + lr + 320)) + (*(LAS f32x2*)(L + lr + 640) + *(LAS f32x2*)(L + lr + 960));
      PUT(a.x, a.y, s_off);
    }
  }
}
}

extern "C" void kernel_launch(void* const* d_in, const int* in_sizes, int n_in, void* d_out, int out_size, void* d_ws, size_t ws_size,
                              hipStream_t stream) {
  Params p;
  p.x = (const unsigned short*)d_in[0];
  p.k = (const unsigned char*)d_in[1];
  p.v = (const unsigned char*)d_in[2];
  p.wq = (const unsigned char*)d_in[3];
  p.wo = (const unsigned char*)d_in[4];
  p.wg = (const unsigned char*)d_in[6];
  p.wu = (const unsigned char*)d_in[7];
  p.wd = (const unsigned char*)d_in[8];
  p.out = (unsigned short*)d_out;
  p.ws = (unsigned char*)d_ws;
  hipLaunchKernelGGL(fused_decode, dim3(256), dim3(320), 0, stream, p.wq, p.wg, p.wu, p.x, p.ws, p.k, p.v, p.wo, p.wd, p.out);
}
```
